```python
import torch

def _softmax(x, axis=-1):
    return torch.softmax(x, dim=axis)
def _gelu(x):
    return torch.nn.functional.gelu(x, approximate="tanh")
def _silu(x):
    return torch.nn.functional.silu(x)
def _sigmoid(x):
    return torch.sigmoid(x)
def _layernorm(x, axis=-1, eps=1e-5):
    m = x.mean(dim=axis, keepdim=True)
    v = x.var(dim=axis, keepdim=True, unbiased=False)
    return (x - m) / torch.sqrt(v + eps)
def _scatter_add(tgt, idx, src, axis=0):
    return tgt.clone().index_add_(axis, idx, src)
def _segment_sum(src, seg, n):
    out = torch.zeros((n,) + src.shape[1:], dtype=src.dtype)
    return out.index_add_(0, seg, src)
def _pad(x, pw):
    flat = tuple(v for pair in reversed(pw) for v in pair)
    return torch.nn.functional.pad(x, flat)

kernel_name = 'sequential_preLN__grouped_kv_mqa__swiglu_ffn__prompt'

def setup_inputs():

    _w = (lambda a: (lambda t: t.to(torch.bfloat16) if t.is_floating_point() else t)(torch.as_tensor(a)))
    def _A_setup():
        B, S, D, H = 1, 2048, 512, 32
        Dh = D // H
        return {
            "x": _w((lambda *s: torch.randn(*s, dtype=torch.bfloat16) * 0.05)(B, S, D)),
            "wq": _w((lambda *s: torch.randn(*s, dtype=torch.bfloat16) * 0.05)(D, H * Dh)),
            "wk": _w((lambda *s: torch.randn(*s, dtype=torch.bfloat16) * 0.05)(D, Dh)),
            "wv": _w((lambda *s: torch.randn(*s, dtype=torch.bfloat16) * 0.05)(D, Dh)),
            "wo": _w((lambda *s: torch.randn(*s, dtype=torch.bfloat16) * 0.05)(H * Dh, D)),
        }
    _A_in = _A_setup()

    def _B_setup():
        B, S, D, F = 1, 2048, 512, 1376
        return {
            "x": _w((lambda *s: torch.randn(*s, dtype=torch.bfloat16) * 0.05)(B, S, D)),
            "w_gate": _w((lambda *s: torch.randn(*s, dtype=torch.bfloat16) * 0.05)(D, F)),
            "w_up": _w((lambda *s: torch.randn(*s, dtype=torch.bfloat16) * 0.05)(D, F)),
            "w_down": _w((lambda *s: torch.randn(*s, dtype=torch.bfloat16) * 0.05)(F, D)),
        }
    _B_in = _B_setup()

    return {
        "A_x": _A_in["x"],
        "A_wq": _A_in["wq"],
        "A_wk": _A_in["wk"],
        "A_wv": _A_in["wv"],
        "A_wo": _A_in["wo"],
        "B_x": _B_in["x"],
        "B_w_gate": _B_in["w_gate"],
        "B_w_up": _B_in["w_up"],
        "B_w_down": _B_in["w_down"],
    }

def reference(A_x, A_wq, A_wk, A_wv, A_wo, B_x, B_w_gate, B_w_up, B_w_down):
    def _A_ref(x, wq, wk, wv, wo):
        B, S, D = x.shape
        H = 32
        Dh = D // H
        q = (lambda x, *d: x.permute(*d))((x @ wq).reshape(B, S, H, Dh), 0, 2, 1, 3)
        k = x @ wk
        v = x @ wv
        scores = torch.einsum("bhqd,bkd->bhqk", q, k) * (Dh ** -0.5)
        p = _softmax(scores, axis=-1)
        o = torch.einsum("bhqk,bkd->bhqd", p, v)
        return (lambda x, *d: x.permute(*d))(o, 0, 2, 1, 3).reshape(B, S, H * Dh) @ wo

    def _B_ref(x, w_gate, w_up, w_down):
        g = _silu(x @ w_gate)
        u = x @ w_up
        return (g * u) @ w_down

    x = A_x
    h = x + _A_ref(_layernorm(x, axis=-1), A_wq, A_wk, A_wv, A_wo)
    h2 = h + _B_ref(_layernorm(h, axis=-1), B_w_gate, B_w_up, B_w_down)
    return h2
```

```cpp
#include <hip/hip_runtime.h>
#include <hip/hip_bf16.h>
#include <stdint.h>
#include <type_traits>

#define DI __device__ __forceinline__
typedef unsigned short u16;
typedef __attribute__((ext_vector_type(8))) __bf16 bf16x8;
typedef __attribute__((ext_vector_type(2))) __bf16 bf16x2;
typedef __attribute__((ext_vector_type(2))) float f32x2;
typedef __attribute__((ext_vector_type(4))) float f32x4;
typedef __attribute__((ext_vector_type(16))) float f32x16;
typedef __attribute__((ext_vector_type(4))) unsigned u32x4;
typedef __attribute__((ext_vector_type(2))) unsigned u32x2;
typedef __attribute__((ext_vector_type(4))) short s16x4;

constexpr int F_ = 1376;
constexpr int NHU = 172;
constexpr int KS4 = 43;

constexpr size_t WS_OX   = 0;
constexpr size_t WS_K    = WS_OX + 2048 * 512 * 2;
constexpr size_t WS_VF   = WS_K + 2048 * 16 * 2;
constexpr size_t WS_NRM  = WS_VF + 2048 * 16 * 2;
constexpr size_t WS_WOF  = WS_NRM + 8192;
constexpr size_t WS_WGU  = WS_WOF + 512 * 512 * 2;
constexpr size_t WS_WD   = WS_WGU + (size_t)NHU * 16 * 1024;
constexpr size_t WS_H    = WS_WD + (size_t)32 * KS4 * 1024;
constexpr size_t WS_HN   = WS_H + 2048 * 512 * 2;
constexpr size_t WS_ACT  = WS_HN + 2048 * 512 * 2;
constexpr size_t WS_CNT  = WS_ACT + (size_t)128 * KS4 * 1024;
constexpr size_t WS_FW = WS_CNT + 256, WS_FH = WS_FW + 2048, WS_FA = WS_FH + 2048;
constexpr size_t WS_FKV = WS_FA + 2048;
constexpr size_t WS_FO = WS_FKV + 16384;
constexpr size_t WS_FOX = WS_FO + 2048;
constexpr size_t WS_END  = WS_FOX + 256 * 128;

DI unsigned pack2(float a, float b) {
  bf16x2 r = __builtin_convertvector((f32x2){a, b}, bf16x2);
  return __builtin_bit_cast(unsigned, r);
}
DI float bfr(float a) {
  return __uint_as_float(pack2(a, 0.f) << 16);
}
DI float bflo(unsigned u) { return __uint_as_float(u << 16); }
DI float bfhi(unsigned u) { return __uint_as_float(u & 0xffff0000u); }
DI f32x4 mfma16(u32x4 a, u32x4 b, f32x4 c) {
  return __builtin_amdgcn_mfma_f32_16x16x32_bf16(__builtin_bit_cast(bf16x8, a), __builtin_bit_cast(bf16x8, b), c, 0, 0, 0);
}
DI f32x16 mfma32(u32x4 a, u32x4 b, f32x16 c) {
  return __builtin_amdgcn_mfma_f32_32x32x16_bf16(__builtin_bit_cast(bf16x8, a), __builtin_bit_cast(bf16x8, b), c, 0, 0, 0);
}
DI float shx(float v, int m) { return __shfl_xor(v, m, 64); }
DI float sum_g(float v) {
  auto a = __builtin_amdgcn_permlane16_swap(__float_as_uint(v), __float_as_uint(v), false, false);
  v = __uint_as_float(a[0]) + __uint_as_float(a[1]);
  auto b = __builtin_amdgcn_permlane32_swap(__float_as_uint(v), __float_as_uint(v), false, false);
  return __uint_as_float(b[0]) + __uint_as_float(b[1]);
}
DI float max_row(float v) {
  unsigned u = __float_as_uint(v);
  u = max(u, (unsigned)__builtin_amdgcn_update_dpp(0, (int)u, 0xB1, 0xf, 0xf, false));
  u = max(u, (unsigned)__builtin_amdgcn_update_dpp(0, (int)u, 0x4E, 0xf, 0xf, false));
  u = max(u, (unsigned)__builtin_amdgcn_update_dpp(0, (int)u, 0x141, 0xf, 0xf, false));
  u = max(u, (unsigned)__builtin_amdgcn_update_dpp(0, (int)u, 0x140, 0xf, 0xf, false));
  return __uint_as_float(u);
}
DI int fresh_lane() {
  int l;
  asm volatile("v_mbcnt_lo_u32_b32 %0, -1, 0\n\tv_mbcnt_hi_u32_b32 %0, -1, %0" : "=v"(l));
  return l;
}

#define LDSP(p) ((__attribute__((address_space(3))) void*)(p))
DI u32x2 tr_read(const char* p) {
  s16x4 v = __builtin_amdgcn_ds_read_tr16_b64_v4i16((__attribute__((address_space(3))) s16x4*)(p));
  return __builtin_bit_cast(u32x2, v);
}
constexpr int NG_GU = 16 * 43, NG_D = 43 * 8, NG_O = 16 * 8;
DI void retile_issue(const u16* __restrict__ wg, const u16* __restrict__ wu, const u16* __restrict__ wd,
                     const u16* __restrict__ wo, int grp, int lane, char* img) {
  const bool isg = grp < NG_GU;
  const int ksg = grp / 43, fb = grp - ksg * 43;
  const bool isd = grp < NG_GU + NG_D;
  const int g2 = isd ? grp - NG_GU : grp - NG_GU - NG_D, ksd = g2 >> 3, cbk = g2 & 7;
  const char* b0 = isg ? (const char*)wg + ((size_t)(ksg * 32) * F_ + 32 * fb) * 2
                       : (const char*)(isd ? wd : wo) + ((size_t)(ksd * 32) * 512 + 64 * cbk) * 2;
  const long du = (const char*)wu - (const char*)wg;
  const unsigned voff = isg ? (unsigned)((lane >> 2) * (F_ * 2) + (lane & 3) * 16) : (unsigned)((lane >> 3) * 1024 + (lane & 7) * 16);
#pragma unroll
  for (int i = 0; i < 4; ++i) {
    const long so = isg ? (long)((i & 1) * 16 * F_ * 2) + ((i >> 1) ? du : 0l) : (long)(i * 8192);
    unsigned keep;
    const unsigned dst = __builtin_amdgcn_readfirstlane((unsigned)(size_t)LDSP(img + i * 1024));
    asm volatile("s_mov_b32 %0, m0\n\ts_mov_b32 m0, %2\n\ts_nop 0\n\tglobal_load_lds_dwordx4 %1, off\n\ts_mov_b32 m0, %0"
                 : "=&s"(keep) : "v"(b0 + so + voff), "s"(dst) : "memory");
  }
}
DI __amdgpu_buffer_rsrc_t ws_rsrc(char* ws) { return __builtin_amdgcn_make_buffer_rsrc(ws, 0, (int)WS_END, 0x00020000); }
DI u32x4 ld_dev(__amdgpu_buffer_rsrc_t r, unsigned voff, unsigned soff) {
  return __builtin_bit_cast(u32x4, __builtin_amdgcn_raw_buffer_load_b128(r, voff, soff, 16));
}
DI void st_dev(__amdgpu_buffer_rsrc_t r, unsigned voff, unsigned soff, u32x4 v) {
  __builtin_amdgcn_raw_buffer_store_b128(v, r, voff, soff, 16);
}
DI void retile_finish(char* __restrict__ ws, int grp, int lane, const char* img) {
  const bool isg = grp < NG_GU;
  const int ksg = grp / 43, fb = grp - ksg * 43;
  const bool isd = grp < NG_GU + NG_D;
  const int g2 = isd ? grp - NG_GU : grp - NG_GU - NG_D, ksd = g2 >> 3, cbk = g2 & 7;
  const int g = lane >> 4, q = (lane >> 2) & 3, p = lane & 3;
  const int rs = isg ? 64 : 128, js = isg ? 16 : 32;
  const char* ra = img + (8 * g + q) * rs + (isg ? (p >> 1) * 2048 + (p & 1) * 8 : p * 8);
  const int d0 = isg ? (int)(WS_WGU / 1024) + ksg * NHU + 4 * fb
                     : (isd ? (int)(WS_WD / 1024) + (4 * cbk) * KS4 + ksd : (int)(WS_WOF / 1024) + ksd * 32 + 4 * cbk);
  const int dstep = (isd && !isg) ? KS4 : 1;
#pragma unroll
  for (int j = 0; j < 4; ++j) {
    const u32x2 lo = tr_read(ra + j * js), hi = tr_read(ra + j * js + 4 * rs);
    st_dev(ws_rsrc(ws), (unsigned)(lane * 16), (unsigned)(d0 + j * dstep) * 1024u, (u32x4){lo[0], lo[1], hi[0], hi[1]});
  }
}

typedef __attribute__((address_space(1))) unsigned gu32;
typedef __attribute__((address_space(1))) unsigned long long gu64;
typedef unsigned long long u64;
DI void wg_bar() { asm volatile("s_waitcnt lgkmcnt(0)\n\ts_barrier" ::: "memory"); }
DI u64 ld_tag(const char* p) { return __hip_atomic_load((gu64*)p, __ATOMIC_RELAXED, __HIP_MEMORY_SCOPE_AGENT); }
DI void st_tag(char* p, u64 v) { __hip_atomic_store((gu64*)p, v, __ATOMIC_RELAXED, __HIP_MEMORY_SCOPE_AGENT); }
DI void st_dev4(char* p, float v) { __hip_atomic_store((gu32*)p, __float_as_uint(v), __ATOMIC_RELAXED, __HIP_MEMORY_SCOPE_AGENT); }
DI float ld_dev4(const char* p) { return __uint_as_float(__hip_atomic_load((gu32*)p, __ATOMIC_RELAXED, __HIP_MEMORY_SCOPE_AGENT)); }
template <int N, int SLEEP = 2>
DI void wait_tags(const char* p, bool want, u64 tag) {
  for (unsigned spin = 0; spin < (1u << 18); ++spin) {
    bool ok = true;
#pragma unroll
    for (int i = 0; i < N; ++i) ok &= (ld_tag(p + i * 512) == tag);
    if (__builtin_amdgcn_ballot_w64(want && !ok) == 0ull) break;
    __builtin_amdgcn_s_sleep(SLEEP);
  }
  asm volatile("" ::: "memory");
}

constexpr int K1_OFF_ST = 65536 + 65536 + 16384;
constexpr int K1_OFF_SYNC = K1_OFF_ST + 512;
constexpr int K1_OFF_IMG = K1_OFF_ST + 1024;
constexpr int K1_LDS = K1_OFF_IMG + 4096;
typedef __attribute__((address_space(3))) volatile unsigned lds_vu32;
DI unsigned lds_inc(char* p) {
  return __hip_atomic_fetch_add((__attribute__((address_space(3))) unsigned*)LDSP(p), 1u, __ATOMIC_RELAXED, __HIP_MEMORY_SCOPE_WORKGROUP);
}
DI void qsync(char* smem, int lane, unsigned target) {
  if (lane == 0) asm volatile("ds_add_u32 %0, %1" :: "v"((unsigned)(size_t)LDSP(smem + K1_OFF_SYNC)), "v"(1u) : "memory");
  lds_vu32* c = (lds_vu32*)LDSP(smem + K1_OFF_SYNC);
  while ((unsigned)__builtin_amdgcn_readfirstlane(*c) < target) __builtin_amdgcn_s_sleep(1);
  asm volatile("" ::: "memory");
}
DI f32x2 ln_stats(float s1, float s2) {
  const float mean = s1 * (1.f / 512.f);
  const float var = fmaxf(s2 * (1.f / 512.f) - mean * mean, 0.f);
  const float sb = bfr(__builtin_amdgcn_sqrtf(bfr(bfr(var) + 1e-5f)));
  return (f32x2){bfr(mean), __builtin_amdgcn_rcpf(sb)};
}
DI float diag16(f32x4 d, int i16, int g) {
  const float e = (i16 & 2) ? ((i16 & 1) ? d[3] : d[2]) : ((i16 & 1) ? d[1] : d[0]);
  return sum_g(g == (i16 >> 2) ? e : 0.f);
}
DI void q_wave(char* smem, const u16* __restrict__ x, const u16* __restrict__ wq, char* __restrict__ ws, int w, int rt, int cb, u64 tag) {
  char* xs = smem;
  char* wsm = smem + 65536;
  float* st = (float*)(smem + K1_OFF_ST);
  const int lane = fresh_lane();
  const int i16 = lane & 15, g = lane >> 4, q = i16 >> 2, p = i16 & 3;
  {
    const int c = lane & 7;
    const int kl = lane >> 3;
    const int sw0 = (kl >> 1) & 1;
#pragma unroll 1
    for (int j = 0; j < 4; ++j) {
      const int row = 16 * w + 4 * j + g;
      const u16* src = x + (size_t)(rt * 64 + row) * 512 + ((i16 ^ (4 * j + g)) * 8);
#pragma unroll
      for (int kc = 0; kc < 4; ++kc)
        __builtin_amdgcn_global_load_lds((const void*)(src + kc * 128), LDSP(xs + kc * 16384 + (16 * w + 4 * j) * 256), 16, 0, 0);
    }
#pragma unroll 1
    for (int kc = 0; kc < 4; ++kc)
#pragma unroll
      for (int j = 0; j < 4; ++j) {
        const int kg = kc * 16 + w * 4 + j;
        const int sw = sw0 | ((j & 1) << 1);
        const int cs = (((c >> 1) ^ sw) << 1) | (c & 1);
        const u16* src = wq + (size_t)(kg * 8 + kl) * 512 + cb * 64 + cs * 8;
        __builtin_amdgcn_global_load_lds((const void*)src, LDSP(wsm + kg * 1024), 16, 0, 0);
      }
  }
  if (w == 0 && lane < 16) *(unsigned*)(smem + K1_OFF_SYNC + lane * 4) = 0u;
  asm volatile("s_waitcnt vmcnt(16) lgkmcnt(0)\n\ts_barrier" ::: "memory");
  f32x4 acc[4], csum;
#pragma unroll
  for (int m = 0; m < 4; ++m) acc[m] = (f32x4){0.f, 0.f, 0.f, 0.f};
  csum = (f32x4){0.f, 0.f, 0.f, 0.f};
  unsigned one2 = 0x3f803f80u;
  asm volatile("" : "+v"(one2));
  const u32x4 ones = {one2, one2, one2, one2};
  const char* xb[4];
#pragma unroll
  for (int m = 0; m < 4; ++m) xb[m] = xs + (16 * ((m + w) & 3) + i16) * 256;
  {
    f32x4 d1 = {0.f, 0.f, 0.f, 0.f}, d2 = {0.f, 0.f, 0.f, 0.f};
#pragma unroll
    for (int ks = 0; ks < 16; ++ks) {
      const u32x4 xf = *(const u32x4*)(xb[0] + (ks >> 2) * 16384 + (((4 * (ks & 3) + g) ^ i16) * 16));
      d1 = mfma16(ones, xf, d1);
      d2 = mfma16(xf, xf, d2);
    }
    const f32x2 sv = ln_stats(d1[0], diag16(d2, i16, g));
    if (g == 0) *(f32x2*)(st + (16 * w + i16) * 2) = sv;
  }
  const int kr0 = 8 * g + q;
  const int sw = ((kr0 >> 1) & 1) | (((kr0 >> 3) & 1) << 1);
  const char* wb = wsm + kr0 * 128 + ((w ^ sw) * 32) + p * 8;
#pragma unroll
  for (int kc = 0; kc < 2; ++kc) {
    if (kc == 0) asm volatile("s_waitcnt vmcnt(8) lgkmcnt(0)" ::: "memory");
    else         asm volatile("s_waitcnt vmcnt(0)" ::: "memory");
    qsync(smem, lane, 4u * (kc + 1));
#pragma unroll
    for (int k8 = 0; k8 < 8; ++k8) {
      const int ks = 8 * kc + k8;
      u32x4 xf[4];
#pragma unroll
      for (int m = 0; m < 4; ++m) xf[m] = *(const u32x4*)(xb[m] + (ks >> 2) * 16384 + (((4 * (ks & 3) + g) ^ i16) * 16));
      const u32x2 lo = tr_read(wb + ks * 32 * 128), hi = tr_read(wb + (ks * 32 + 4) * 128);
      const u32x4 wf = {lo[0], lo[1], hi[0], hi[1]};
#pragma unroll
      for (int m = 0; m < 4; ++m) acc[m] = mfma16(wf, xf[m], acc[m]);
      csum = mfma16(wf, ones, csum);
    }
  }
  char* qs = wsm;
  float nmax = 0.f;
#pragma unroll
  for (int m = 0; m < 4; ++m) {
    const int mm = (m + w) & 3;
    const f32x2 sv = *(const f32x2*)(st + (16 * mm + i16) * 2);
    const float mb = sv[0], rs = sv[1] * (0.25f * 1.4426950408889634f);
    float v[4];
#pragma unroll
    for (int r = 0; r < 4; ++r) v[r] = (acc[m][r] - mb * csum[r]) * rs;
    *(u32x2*)(qs + (2 * mm + (i16 >> 3)) * 1024 + ((g >> 1) * 32 + w * 8 + (i16 & 7)) * 16 + (g & 1) * 8) = (u32x2){pack2(v[0], v[1]), pack2(v[2], v[3])};
    const float ss = sum_g(v[0] * v[0] + v[1] * v[1] + v[2] * v[2] + v[3] * v[3]);
    nmax = __uint_as_float(max(__float_as_uint(nmax), __float_as_uint(ss) & 0x7fffffffu));
  }
  nmax = max_row(nmax);
  if (lane == 0) *(float*)(smem + K1_OFF_SYNC + 32 + w * 4) = nmax;
  asm volatile("s_waitcnt lgkmcnt(0)" ::: "memory");
  qsync(smem, lane, 12u);
}
template <bool ISV>
DI void kv_body(char* smem, char* __restrict__ ws, int m, int lane, int rt) {
  const char* xs = smem;
  const char* kvs = smem + 131072;
  const int i16 = lane & 15, g = lane >> 4, q = i16 >> 2, p = i16 & 3;
  unsigned one2 = 0x3f803f80u;
  asm volatile("" : "+v"(one2));
  const u32x4 ones = {one2, one2, one2, one2};
  f32x4 acc = {0.f, 0.f, 0.f, 0.f}, csum = {0.f, 0.f, 0.f, 0.f};
  f32x4 d1 = {0.f, 0.f, 0.f, 0.f}, d2 = {0.f, 0.f, 0.f, 0.f};
  const char* xb = xs + (16 * m + i16) * 256;
  const char* wb = kvs + (8 * g + q) * 32 + p * 8;
#pragma unroll 1
  for (int kc = 0; kc < 4; ++kc) {
#pragma unroll
    for (int k4 = 0; k4 < 4; ++k4) {
      const u32x4 xf = *(const u32x4*)(xb + kc * 16384 + (((4 * k4 + g) ^ i16) * 16));
      const u32x2 lo = tr_read(wb + kc * 4096 + k4 * 1024), hi = tr_read(wb + kc * 4096 + k4 * 1024 + 128);
      const u32x4 wf = {lo[0], lo[1], hi[0], hi[1]};
      if (ISV) { acc = mfma16(xf, wf, acc); csum = mfma16(ones, wf, csum); }
      else     { acc = mfma16(wf, xf, acc); csum = mfma16(wf, ones, csum); }
      d1 = mfma16(ones, xf, d1);
      d2 = mfma16(xf, xf, d2);
    }
  }
  const f32x2 sv = ln_stats(d1[0], diag16(d2, i16, g));
  float v[4], nv;
  if (!ISV) {
#pragma unroll
    for (int r = 0; r < 4; ++r) v[r] = (acc[r] - sv[0] * csum[r]) * sv[1];
    st_tag(ws + WS_K + (size_t)(rt * 2 + (m >> 1)) * 1024 + ((g >> 1) * 32 + (m & 1) * 16 + i16) * 16 + (g & 1) * 8,
           (u64)pack2(v[0], v[1]) | ((u64)pack2(v[2], v[3]) << 32));
    nv = sum_g(v[0] * v[0] + v[1] * v[1] + v[2] * v[2] + v[3] * v[3]);
  } else {
#pragma unroll
    for (int r = 0; r < 4; ++r) {
      const float mb = __shfl(sv[0], 4 * g + r, 64), rs = __shfl(sv[1], 4 * g + r, 64);
      v[r] = (acc[r] - mb * csum[r]) * rs;
    }
    const int kt = rt * 2 + (m >> 1), s = m & 1;
    st_tag(ws + WS_VF + ((size_t)(((kt * 2 + (g & 1)) * 2 + s) * 16 + i16) * 8 + 4 * (g >> 1)) * 2,
           (u64)pack2(v[0], v[1]) | ((u64)pack2(v[2], v[3]) << 32));
    nv = sum_g((fabsf(v[0]) + fabsf(v[1])) + (fabsf(v[2]) + fabsf(v[3])));
  }
  nv = max_row(__uint_as_float(__float_as_uint(nv) & 0x7fffffffu));
  if (lane == 0) st_dev4(ws + WS_NRM + (1024 + (ISV ? 128 : 0) + rt * 4 + m) * 4, nv);
}
DI void kv_wave(char* smem, const u16* __restrict__ wk, const u16* __restrict__ wv, const u16* __restrict__ wo,
                const u16* __restrict__ wd, char* __restrict__ ws, int aw, int rt, int cb, u64 tag) {
  const int lane = fresh_lane();
  const int jb = rt * 8 + cb;
  const bool job = (aw == 1) && (jb < NG_O + 8);
  {
    const u16* wsrc = (cb & 1) ? wv : wk;
#pragma unroll
    for (int r = 0; r < 4; ++r)
      __builtin_amdgcn_global_load_lds((const void*)(wsrc + (aw * 4 + r) * 512 + lane * 8), LDSP(smem + 131072 + (aw * 4 + r) * 1024), 16, 0, 0);
    asm volatile("s_waitcnt vmcnt(0)" ::: "memory");
  }
  if (job) retile_issue(wd, wd, wd, wo, 1024 + jb, lane, smem + K1_OFF_IMG);
  asm volatile("s_barrier" ::: "memory");
  lds_vu32* ok = (lds_vu32*)LDSP(smem + K1_OFF_SYNC + 12);
  if (aw == 0) {
    __builtin_amdgcn_s_setprio(3);
    if (cb & 1) kv_body<true>(smem, ws, cb >> 1, lane, rt);
    else kv_body<false>(smem, ws, cb >> 1, lane, rt);
    asm volatile("s_waitcnt vmcnt(0)" ::: "memory");
    if (lane < 8) st_tag(ws + WS_FKV + (lane * 256 + rt * 8 + cb) * 8, tag);
    __builtin_amdgcn_s_setprio(0);
    wait_tags<4>(ws + WS_FKV + ((blockIdx.x & 7) * 256 + lane) * 8, true, tag);
    *ok = 1u;
  } else {
    if (job) {
      asm volatile("s_waitcnt vmcnt(0)" ::: "memory");
      retile_finish(ws, 1024 + jb, lane, smem + K1_OFF_IMG);
      asm volatile("s_waitcnt vmcnt(0)" ::: "memory");
      if (lane == 0) st_tag(ws + WS_FO + jb * 8, tag);
    }
    while (__builtin_amdgcn_readfirstlane(*ok) == 0u) __builtin_amdgcn_s_sleep(1);
    asm volatile("" ::: "memory");
  }
}

constexpr int PROW = 96;
constexpr int PART_STRIDE = 32 * PROW;
constexpr int K2_OFF_PART = 0;
constexpr int K2_OFF_OBUF = K2_OFF_PART + 32 * PART_STRIDE;
constexpr int K2_OFF_RED = K2_OFF_OBUF + 8 * 1024;
constexpr int K2_OFF_FLAG = K2_OFF_RED + 1024;
constexpr int K2_OFF_IMG = K2_OFF_FLAG + 512;
constexpr int K2_OFF_Q = K2_OFF_IMG + 16384;
constexpr int K2_OFF_DUMP = K2_OFF_Q + 8192;
constexpr int K2_OFF_X = K2_OFF_DUMP + 4096;
constexpr int K2_LDS = K2_OFF_X + 8192;

#define ATTN_WAVE_ASM \
  "s_mov_b64 %[t0], %[kb]\n\t" \
  "s_mov_b64 %[t1], %[vb]\n\t" \
  "global_load_dwordx4 a[0:3], v1, %[t0] offset:0 sc1\n\t" \
  "s_mov_b64 exec, %[msk]\n\t" \
  "global_load_dwordx4 a[64:67], v1, %[t1] offset:0 sc1\n\t" \
  "global_load_dwordx4 a[68:71], v1, %[t1] offset:256 sc1\n\t" \
  "s_mov_b64 exec, -1\n\t" \
  "global_load_dwordx4 a[4:7], v1, %[t0] offset:1024 sc1\n\t" \
  "s_mov_b64 exec, %[msk]\n\t" \
  "global_load_dwordx4 a[72:75], v1, %[t1] offset:1024 sc1\n\t" \
  "global_load_dwordx4 a[76:79], v1, %[t1] offset:1280 sc1\n\t" \
  "s_mov_b64 exec, -1\n\t" \
  "global_load_dwordx4 a[8:11], v1, %[t0] offset:2048 sc1\n\t" \
  "s_mov_b64 exec, %[msk]\n\t" \
  "global_load_dwordx4 a[80:83], v1, %[t1] offset:2048 sc1\n\t" \
  "global_load_dwordx4 a[84:87], v1, %[t1] offset:2304 sc1\n\t" \
  "s_mov_b64 exec, -1\n\t" \
  "global_load_dwordx4 a[12:15], v1, %[t0] offset:3072 sc1\n\t" \
  "s_mov_b64 exec, %[msk]\n\t" \
  "global_load_dwordx4 a[88:91], v1, %[t1] offset:3072 sc1\n\t" \
  "global_load_dwordx4 a[92:95], v1, %[t1] offset:3328 sc1\n\t" \
  "s_mov_b64 exec, -1\n\t" \
  "v_add_u32 v1, 0x1000, v1\n\t" \
  "global_load_dwordx4 a[16:19], v1, %[t0] offset:0 sc1\n\t" \
  "s_mov_b64 exec, %[msk]\n\t" \
  "global_load_dwordx4 a[96:99], v1, %[t1] offset:0 sc1\n\t" \
  "global_load_dwordx4 a[100:103], v1, %[t1] offset:256 sc1\n\t" \
  "s_mov_b64 exec, -1\n\t" \
  "global_load_dwordx4 a[20:23], v1, %[t0] offset:1024 sc1\n\t" \
  "s_mov_b64 exec, %[msk]\n\t" \
  "global_load_dwordx4 a[104:107], v1, %[t1] offset:1024 sc1\n\t" \
  "global_load_dwordx4 a[108:111], v1, %[t1] offset:1280 sc1\n\t" \
  "s_mov_b64 exec, -1\n\t" \
  "s_mov_b32 exec_lo, 0xffff0000\n\t" \
  "s_mov_b32 exec_hi, 0xffff0000\n\t" \
  "v_accvgpr_write_b32 a64, v26\n\t" \
  "v_accvgpr_write_b32 a65, v26\n\t" \
  "v_accvgpr_write_b32 a66, v26\n\t" \
  "v_accvgpr_write_b32 a67, v26\n\t" \
  "v_accvgpr_write_b32 a68, v26\n\t" \
  "v_accvgpr_write_b32 a69, v26\n\t" \
  "v_accvgpr_write_b32 a70, v26\n\t" \
  "v_accvgpr_write_b32 a71, v26\n\t" \
  "v_accvgpr_write_b32 a72, v26\n\t" \
  "v_accvgpr_write_b32 a73, v26\n\t" \
  "v_accvgpr_write_b32 a74, v26\n\t" \
  "v_accvgpr_write_b32 a75, v26\n\t" \
  "v_accvgpr_write_b32 a76, v26\n\t" \
  "v_accvgpr_write_b32 a77, v26\n\t" \
  "v_accvgpr_write_b32 a78, v26\n\t" \
  "v_accvgpr_write_b32 a79, v26\n\t" \
  "v_accvgpr_write_b32 a80, v26\n\t" \
  "v_accvgpr_write_b32 a81, v26\n\t" \
  "v_accvgpr_write_b32 a82, v26\n\t" \
  "v_accvgpr_write_b32 a83, v26\n\t" \
  "v_accvgpr_write_b32 a84, v26\n\t" \
  "v_accvgpr_write_b32 a85, v26\n\t" \
  "v_accvgpr_write_b32 a86, v26\n\t" \
  "v_accvgpr_write_b32 a87, v26\n\t" \
  "v_accvgpr_write_b32 a88, v26\n\t" \
  "v_accvgpr_write_b32 a89, v26\n\t" \
  "v_accvgpr_write_b32 a90, v26\n\t" \
  "v_accvgpr_write_b32 a91, v26\n\t" \
  "v_accvgpr_write_b32 a92, v26\n\t" \
  "v_accvgpr_write_b32 a93, v26\n\t" \
  "v_accvgpr_write_b32 a94, v26\n\t" \
  "v_accvgpr_write_b32 a95, v26\n\t" \
  "v_accvgpr_write_b32 a96, v26\n\t" \
  "v_accvgpr_write_b32 a97, v26\n\t" \
  "v_accvgpr_write_b32 a98, v26\n\t" \
  "v_accvgpr_write_b32 a99, v26\n\t" \
  "v_accvgpr_write_b32 a100, v26\n\t" \
  "v_accvgpr_write_b32 a101, v26\n\t" \
  "v_accvgpr_write_b32 a102, v26\n\t" \
  "v_accvgpr_write_b32 a103, v26\n\t" \
  "v_accvgpr_write_b32 a104, v26\n\t" \
  "v_accvgpr_write_b32 a105, v26\n\t" \
  "v_accvgpr_write_b32 a106, v26\n\t" \
  "v_accvgpr_write_b32 a107, v26\n\t" \
  "v_accvgpr_write_b32 a108, v26\n\t" \
  "v_accvgpr_write_b32 a109, v26\n\t" \
  "v_accvgpr_write_b32 a110, v26\n\t" \
  "v_accvgpr_write_b32 a111, v26\n\t" \
  "v_accvgpr_write_b32 a112, v26\n\t" \
  "v_accvgpr_write_b32 a113, v26\n\t" \
  "v_accvgpr_write_b32 a114, v26\n\t" \
  "v_accvgpr_write_b32 a115, v26\n\t" \
  "v_accvgpr_write_b32 a116, v26\n\t" \
  "v_accvgpr_write_b32 a117, v26\n\t" \
  "v_accvgpr_write_b32 a118, v26\n\t" \
  "v_accvgpr_write_b32 a119, v26\n\t" \
  "v_accvgpr_write_b32 a120, v26\n\t" \
  "v_accvgpr_write_b32 a121, v26\n\t" \
  "v_accvgpr_write_b32 a122, v26\n\t" \
  "v_accvgpr_write_b32 a123, v26\n\t" \
  "v_accvgpr_write_b32 a124, v26\n\t" \
  "v_accvgpr_write_b32 a125, v26\n\t" \
  "v_accvgpr_write_b32 a126, v26\n\t" \
  "v_accvgpr_write_b32 a127, v26\n\t" \
  "v_mov_b32 v64, v26\n\t" \
  "v_mov_b32 v65, v26\n\t" \
  "v_mov_b32 v66, v26\n\t" \
  "v_mov_b32 v67, v26\n\t" \
  "v_mov_b32 v68, v26\n\t" \
  "v_mov_b32 v69, v26\n\t" \
  "v_mov_b32 v70, v26\n\t" \
  "v_mov_b32 v71, v26\n\t" \
  "v_mov_b32 v72, v26\n\t" \
  "v_mov_b32 v73, v26\n\t" \
  "v_mov_b32 v74, v26\n\t" \
  "v_mov_b32 v75, v26\n\t" \
  "v_mov_b32 v76, v26\n\t" \
  "v_mov_b32 v77, v26\n\t" \
  "v_mov_b32 v78, v26\n\t" \
  "v_mov_b32 v79, v26\n\t" \
  "v_mov_b32 v80, v26\n\t" \
  "v_mov_b32 v81, v26\n\t" \
  "v_mov_b32 v82, v26\n\t" \
  "v_mov_b32 v83, v26\n\t" \
  "v_mov_b32 v84, v26\n\t" \
  "v_mov_b32 v85, v26\n\t" \
  "v_mov_b32 v86, v26\n\t" \
  "v_mov_b32 v87, v26\n\t" \
  "v_mov_b32 v88, v26\n\t" \
  "v_mov_b32 v89, v26\n\t" \
  "v_mov_b32 v90, v26\n\t" \
  "v_mov_b32 v91, v26\n\t" \
  "v_mov_b32 v92, v26\n\t" \
  "v_mov_b32 v93, v26\n\t" \
  "v_mov_b32 v94, v26\n\t" \
  "v_mov_b32 v95, v26\n\t" \
  "v_mov_b32 v96, v26\n\t" \
  "v_mov_b32 v97, v26\n\t" \
  "v_mov_b32 v98, v26\n\t" \
  "v_mov_b32 v99, v26\n\t" \
  "v_mov_b32 v100, v26\n\t" \
  "v_mov_b32 v101, v26\n\t" \
  "v_mov_b32 v102, v26\n\t" \
  "v_mov_b32 v103, v26\n\t" \
  "v_mov_b32 v104, v26\n\t" \
  "v_mov_b32 v105, v26\n\t" \
  "v_mov_b32 v106, v26\n\t" \
  "v_mov_b32 v107, v26\n\t" \
  "v_mov_b32 v108, v26\n\t" \
  "v_mov_b32 v109, v26\n\t" \
  "v_mov_b32 v110, v26\n\t" \
  "v_mov_b32 v111, v26\n\t" \
  "v_mov_b32 v112, v26\n\t" \
  "v_mov_b32 v113, v26\n\t" \
  "v_mov_b32 v114, v26\n\t" \
  "v_mov_b32 v115, v26\n\t" \
  "v_mov_b32 v116, v26\n\t" \
  "v_mov_b32 v117, v26\n\t" \
  "v_mov_b32 v118, v26\n\t" \
  "v_mov_b32 v119, v26\n\t" \
  "v_mov_b32 v120, v26\n\t" \
  "v_mov_b32 v121, v26\n\t" \
  "v_mov_b32 v122, v26\n\t" \
  "v_mov_b32 v123, v26\n\t" \
  "v_mov_b32 v124, v26\n\t" \
  "v_mov_b32 v125, v26\n\t" \
  "v_mov_b32 v126, v26\n\t" \
  "v_mov_b32 v127, v26\n\t" \
  "s_mov_b64 exec, -1\n\t" \
  "s_barrier\n\t" \
  "ds_read_b128 v[4:7], v2\n\t" \
  "v_add_u32 v2, 0x400, v2\n\t" \
  "s_waitcnt lgkmcnt(0)\n\t" \
  "s_waitcnt vmcnt(17)\n\t" \
  "v_mfma_f32_32x32x16_bf16 v[24:39], a[0:3], v[4:7], 0\n\t" \
  "s_nop 7\n\t" \
  "s_nop 5\n\t" \
  ".p2align 3\n\t" \
  "s_waitcnt vmcnt(14)\n\t" \
  "s_nop 0\n\t" \
  "global_load_dwordx4 a[24:27], v1, %[t0] offset:2048 sc1\n\t" \
  "s_mov_b64 exec, %[msk]\n\t" \
  "global_load_dwordx4 a[112:115], v1, %[t1] offset:2048 sc1\n\t" \
  "global_load_dwordx4 a[116:119], v1, %[t1] offset:2304 sc1\n\t" \
  "s_mov_b64 exec, -1\n\t" \
  "v_exp_f32 v24, v24\n\t" \
  "v_exp_f32 v25, v25\n\t" \
  "v_exp_f32 v26, v26\n\t" \
  "v_exp_f32 v27, v27\n\t" \
  "v_cvt_pk_bf16_f32 v56, v24, v25\n\t" \
  "v_exp_f32 v28, v28\n\t" \
  "v_exp_f32 v29, v29\n\t" \
  "v_cvt_pk_bf16_f32 v57, v26, v27\n\t" \
  "v_exp_f32 v30, v30\n\t" \
  "v_exp_f32 v31, v31\n\t" \
  "v_cvt_pk_bf16_f32 v58, v28, v29\n\t" \
  "v_exp_f32 v32, v32\n\t" \
  "v_exp_f32 v33, v33\n\t" \
  "v_cvt_pk_bf16_f32 v59, v30, v31\n\t" \
  "v_mfma_f32_32x32x16_bf16 v[40:55], a[4:7], v[4:7], 0\n\t" \
  "v_exp_f32 v34, v34\n\t" \
  "v_exp_f32 v35, v35\n\t" \
  "v_cvt_pk_bf16_f32 v60, v32, v33\n\t" \
  "v_exp_f32 v36, v36\n\t" \
  "v_exp_f32 v37, v37\n\t" \
  "v_cvt_pk_bf16_f32 v61, v34, v35\n\t" \
  "v_mfma_f32_32x32x16_bf16 v[8:23], a[64:67], v[56:59], 0\n\t" \
  "v_exp_f32 v38, v38\n\t" \
  "v_exp_f32 v39, v39\n\t" \
  "v_cvt_pk_bf16_f32 v62, v36, v37\n\t" \
  "s_waitcnt vmcnt(14)\n\t" \
  "s_nop 0\n\t" \
  "global_load_dwordx4 a[28:31], v1, %[t0] offset:3072 sc1\n\t" \
  "s_mov_b64 exec, %[msk]\n\t" \
  "global_load_dwordx4 a[120:123], v1, %[t1] offset:3072 sc1\n\t" \
  "global_load_dwordx4 a[124:127], v1, %[t1] offset:3328 sc1\n\t" \
  "s_mov_b64 exec, -1\n\t" \
  "v_add_u32 v1, 0x1000, v1\n\t" \
  "v_exp_f32 v40, v40\n\t" \
  "v_exp_f32 v41, v41\n\t" \
  "v_cvt_pk_bf16_f32 v63, v38, v39\n\t" \
  "v_exp_f32 v42, v42\n\t" \
  "v_exp_f32 v43, v43\n\t" \
  "v_cvt_pk_bf16_f32 v56, v40, v41\n\t" \
  "v_exp_f32 v44, v44\n\t" \
  "v_exp_f32 v45, v45\n\t" \
  "v_cvt_pk_bf16_f32 v57, v42, v43\n\t" \
  "v_mfma_f32_32x32x16_bf16 v[8:23], a[68:71], v[60:63], v[8:23]\n\t" \
  "v_exp_f32 v46, v46\n\t" \
  "v_exp_f32 v47, v47\n\t" \
  "v_cvt_pk_bf16_f32 v58, v44, v45\n\t" \
  "v_exp_f32 v48, v48\n\t" \
  "v_exp_f32 v49, v49\n\t" \
  "v_cvt_pk_bf16_f32 v59, v46, v47\n\t" \
  "v_mfma_f32_32x32x16_bf16 v[24:39], a[8:11], v[4:7], 0\n\t" \
  "v_exp_f32 v50, v50\n\t" \
  "v_exp_f32 v51, v51\n\t" \
  "v_cvt_pk_bf16_f32 v60, v48, v49\n\t" \
  "v_exp_f32 v52, v52\n\t" \
  "v_exp_f32 v53, v53\n\t" \
  "v_cvt_pk_bf16_f32 v61, v50, v51\n\t" \
  "v_mfma_f32_32x32x16_bf16 v[8:23], a[72:75], v[56:59], v[8:23]\n\t" \
  "v_exp_f32 v54, v54\n\t" \
  "v_exp_f32 v55, v55\n\t" \
  "v_cvt_pk_bf16_f32 v62, v52, v53\n\t" \
  "s_waitcnt vmcnt(14)\n\t" \
  "s_nop 0\n\t" \
  "global_load_dwordx4 a[32:35], v1, %[t0] offset:0 sc1\n\t" \
  "s_mov_b64 exec, %[msk]\n\t" \
  "global_load_dwordx4 v[64:67], v1, %[t1] offset:0 sc1\n\t" \
  "global_load_dwordx4 v[68:71], v1, %[t1] offset:256 sc1\n\t" \
  "s_mov_b64 exec, -1\n\t" \
  "v_exp_f32 v24, v24\n\t" \
  "v_exp_f32 v25, v25\n\t" \
  "v_cvt_pk_bf16_f32 v63, v54, v55\n\t" \
  "v_exp_f32 v26, v26\n\t" \
  "v_exp_f32 v27, v27\n\t" \
  "v_cvt_pk_bf16_f32 v56, v24, v25\n\t" \
  "v_exp_f32 v28, v28\n\t" \
  "v_exp_f32 v29, v29\n\t" \
  "v_cvt_pk_bf16_f32 v57, v26, v27\n\t" \
  "v_mfma_f32_32x32x16_bf16 v[8:23], a[76:79], v[60:63], v[8:23]\n\t" \
  "v_exp_f32 v30, v30\n\t" \
  "v_exp_f32 v31, v31\n\t" \
  "v_cvt_pk_bf16_f32 v58, v28, v29\n\t" \
  "v_exp_f32 v32, v32\n\t" \
  "v_exp_f32 v33, v33\n\t" \
  "v_cvt_pk_bf16_f32 v59, v30, v31\n\t" \
  "v_mfma_f32_32x32x16_bf16 v[40:55], a[12:15], v[4:7], 0\n\t" \
  "v_exp_f32 v34, v34\n\t" \
  "v_exp_f32 v35, v35\n\t" \
  "v_cvt_pk_bf16_f32 v60, v32, v33\n\t" \
  "v_exp_f32 v36, v36\n\t" \
  "v_exp_f32 v37, v37\n\t" \
  "v_cvt_pk_bf16_f32 v61, v34, v35\n\t" \
  "v_mfma_f32_32x32x16_bf16 v[8:23], a[80:83], v[56:59], v[8:23]\n\t" \
  "v_exp_f32 v38, v38\n\t" \
  "v_exp_f32 v39, v39\n\t" \
  "v_cvt_pk_bf16_f32 v62, v36, v37\n\t" \
  "s_waitcnt vmcnt(14)\n\t" \
  "s_nop 0\n\t" \
  "global_load_dwordx4 a[36:39], v1, %[t0] offset:1024 sc1\n\t" \
  "s_mov_b64 exec, %[msk]\n\t" \
  "global_load_dwordx4 v[72:75], v1, %[t1] offset:1024 sc1\n\t" \
  "global_load_dwordx4 v[76:79], v1, %[t1] offset:1280 sc1\n\t" \
  "s_mov_b64 exec, -1\n\t" \
  "v_exp_f32 v40, v40\n\t" \
  "v_exp_f32 v41, v41\n\t" \
  "v_cvt_pk_bf16_f32 v63, v38, v39\n\t" \
  "v_exp_f32 v42, v42\n\t" \
  "v_exp_f32 v43, v43\n\t" \
  "v_cvt_pk_bf16_f32 v56, v40, v41\n\t" \
  "v_exp_f32 v44, v44\n\t" \
  "v_exp_f32 v45, v45\n\t" \
  "v_cvt_pk_bf16_f32 v57, v42, v43\n\t" \
  "v_mfma_f32_32x32x16_bf16 v[8:23], a[84:87], v[60:63], v[8:23]\n\t" \
  "v_exp_f32 v46, v46\n\t" \
  "v_exp_f32 v47, v47\n\t" \
  "v_cvt_pk_bf16_f32 v58, v44, v45\n\t" \
  "v_exp_f32 v48, v48\n\t" \
  "v_exp_f32 v49, v49\n\t" \
  "v_cvt_pk_bf16_f32 v59, v46, v47\n\t" \
  "v_mfma_f32_32x32x16_bf16 v[24:39], a[16:19], v[4:7], 0\n\t" \
  "v_exp_f32 v50, v50\n\t" \
  "v_exp_f32 v51, v51\n\t" \
  "v_cvt_pk_bf16_f32 v60, v48, v49\n\t" \
  "v_exp_f32 v52, v52\n\t" \
  "v_exp_f32 v53, v53\n\t" \
  "v_cvt_pk_bf16_f32 v61, v50, v51\n\t" \
  "v_mfma_f32_32x32x16_bf16 v[8:23], a[88:91], v[56:59], v[8:23]\n\t" \
  "v_exp_f32 v54, v54\n\t" \
  "v_exp_f32 v55, v55\n\t" \
  "v_cvt_pk_bf16_f32 v62, v52, v53\n\t" \
  "s_waitcnt vmcnt(14)\n\t" \
  "s_nop 0\n\t" \
  "global_load_dwordx4 a[40:43], v1, %[t0] offset:2048 sc1\n\t" \
  "s_mov_b64 exec, %[msk]\n\t" \
  "global_load_dwordx4 v[80:83], v1, %[t1] offset:2048 sc1\n\t" \
  "global_load_dwordx4 v[84:87], v1, %[t1] offset:2304 sc1\n\t" \
  "s_mov_b64 exec, -1\n\t" \
  "v_exp_f32 v24, v24\n\t" \
  "v_exp_f32 v25, v25\n\t" \
  "v_cvt_pk_bf16_f32 v63, v54, v55\n\t" \
  "v_exp_f32 v26, v26\n\t" \
  "v_exp_f32 v27, v27\n\t" \
  "v_cvt_pk_bf16_f32 v56, v24, v25\n\t" \
  "v_exp_f32 v28, v28\n\t" \
  "v_exp_f32 v29, v29\n\t" \
  "v_cvt_pk_bf16_f32 v57, v26, v27\n\t" \
  "v_mfma_f32_32x32x16_bf16 v[8:23], a[92:95], v[60:63], v[8:23]\n\t" \
  "v_exp_f32 v30, v30\n\t" \
  "v_exp_f32 v31, v31\n\t" \
  "v_cvt_pk_bf16_f32 v58, v28, v29\n\t" \
  "v_exp_f32 v32, v32\n\t" \
  "v_exp_f32 v33, v33\n\t" \
  "v_cvt_pk_bf16_f32 v59, v30, v31\n\t" \
  "v_mfma_f32_32x32x16_bf16 v[40:55], a[20:23], v[4:7], 0\n\t" \
  "v_exp_f32 v34, v34\n\t" \
  "v_exp_f32 v35, v35\n\t" \
  "v_cvt_pk_bf16_f32 v60, v32, v33\n\t" \
  "v_exp_f32 v36, v36\n\t" \
  "v_exp_f32 v37, v37\n\t" \
  "v_cvt_pk_bf16_f32 v61, v34, v35\n\t" \
  "v_mfma_f32_32x32x16_bf16 v[8:23], a[96:99], v[56:59], v[8:23]\n\t" \
  "v_exp_f32 v38, v38\n\t" \
  "v_exp_f32 v39, v39\n\t" \
  "v_cvt_pk_bf16_f32 v62, v36, v37\n\t" \
  "s_waitcnt vmcnt(14)\n\t" \
  "s_nop 0\n\t" \
  "global_load_dwordx4 a[44:47], v1, %[t0] offset:3072 sc1\n\t" \
  "s_mov_b64 exec, %[msk]\n\t" \
  "global_load_dwordx4 v[88:91], v1, %[t1] offset:3072 sc1\n\t" \
  "global_load_dwordx4 v[92:95], v1, %[t1] offset:3328 sc1\n\t" \
  "s_mov_b64 exec, -1\n\t" \
  "v_add_u32 v1, 0x1000, v1\n\t" \
  "v_exp_f32 v40, v40\n\t" \
  "v_exp_f32 v41, v41\n\t" \
  "v_cvt_pk_bf16_f32 v63, v38, v39\n\t" \
  "v_exp_f32 v42, v42\n\t" \
  "v_exp_f32 v43, v43\n\t" \
  "v_cvt_pk_bf16_f32 v56, v40, v41\n\t" \
  "v_exp_f32 v44, v44\n\t" \
  "v_exp_f32 v45, v45\n\t" \
  "v_cvt_pk_bf16_f32 v57, v42, v43\n\t" \
  "v_mfma_f32_32x32x16_bf16 v[8:23], a[100:103], v[60:63], v[8:23]\n\t" \
  "v_exp_f32 v46, v46\n\t" \
  "v_exp_f32 v47, v47\n\t" \
  "v_cvt_pk_bf16_f32 v58, v44, v45\n\t" \
  "v_exp_f32 v48, v48\n\t" \
  "v_exp_f32 v49, v49\n\t" \
  "v_cvt_pk_bf16_f32 v59, v46, v47\n\t" \
  "v_mfma_f32_32x32x16_bf16 v[24:39], a[24:27], v[4:7], 0\n\t" \
  "v_exp_f32 v50, v50\n\t" \
  "v_exp_f32 v51, v51\n\t" \
  "v_cvt_pk_bf16_f32 v60, v48, v49\n\t" \
  "v_exp_f32 v52, v52\n\t" \
  "v_exp_f32 v53, v53\n\t" \
  "v_cvt_pk_bf16_f32 v61, v50, v51\n\t" \
  "v_mfma_f32_32x32x16_bf16 v[8:23], a[104:107], v[56:59], v[8:23]\n\t" \
  "v_exp_f32 v54, v54\n\t" \
  "v_exp_f32 v55, v55\n\t" \
  "v_cvt_pk_bf16_f32 v62, v52, v53\n\t" \
  "s_waitcnt vmcnt(14)\n\t" \
  "s_nop 0\n\t" \
  "global_load_dwordx4 a[48:51], v1, %[t0] offset:0 sc1\n\t" \
  "s_mov_b64 exec, %[msk]\n\t" \
  "global_load_dwordx4 v[96:99], v1, %[t1] offset:0 sc1\n\t" \
  "global_load_dwordx4 v[100:103], v1, %[t1] offset:256 sc1\n\t" \
  "s_mov_b64 exec, -1\n\t" \
  "v_exp_f32 v24, v24\n\t" \
  "v_exp_f32 v25, v25\n\t" \
  "v_cvt_pk_bf16_f32 v63, v54, v55\n\t" \
  "v_exp_f32 v26, v26\n\t" \
  "v_exp_f32 v27, v27\n\t" \
  "v_cvt_pk_bf16_f32 v56, v24, v25\n\t" \
  "v_exp_f32 v28, v28\n\t" \
  "v_exp_f32 v29, v29\n\t" \
  "v_cvt_pk_bf16_f32 v57, v26, v27\n\t" \
  "v_mfma_f32_32x32x16_bf16 v[8:23], a[108:111], v[60:63], v[8:23]\n\t" \
  "v_exp_f32 v30, v30\n\t" \
  "v_exp_f32 v31, v31\n\t" \
  "v_cvt_pk_bf16_f32 v58, v28, v29\n\t" \
  "v_exp_f32 v32, v32\n\t" \
  "v_exp_f32 v33, v33\n\t" \
  "v_cvt_pk_bf16_f32 v59, v30, v31\n\t" \
  "v_mfma_f32_32x32x16_bf16 v[40:55], a[28:31], v[4:7], 0\n\t" \
  "v_exp_f32 v34, v34\n\t" \
  "v_exp_f32 v35, v35\n\t" \
  "v_cvt_pk_bf16_f32 v60, v32, v33\n\t" \
  "v_exp_f32 v36, v36\n\t" \
  "v_exp_f32 v37, v37\n\t" \
  "v_cvt_pk_bf16_f32 v61, v34, v35\n\t" \
  "v_mfma_f32_32x32x16_bf16 v[8:23], a[112:115], v[56:59], v[8:23]\n\t" \
  "v_exp_f32 v38, v38\n\t" \
  "v_exp_f32 v39, v39\n\t" \
  "v_cvt_pk_bf16_f32 v62, v36, v37\n\t" \
  "s_waitcnt vmcnt(14)\n\t" \
  "s_nop 0\n\t" \
  "global_load_dwordx4 a[52:55], v1, %[t0] offset:1024 sc1\n\t" \
  "s_mov_b64 exec, %[msk]\n\t" \
  "global_load_dwordx4 v[104:107], v1, %[t1] offset:1024 sc1\n\t" \
  "global_load_dwordx4 v[108:111], v1, %[t1] offset:1280 sc1\n\t" \
  "s_mov_b64 exec, -1\n\t" \
  "v_exp_f32 v40, v40\n\t" \
  "v_exp_f32 v41, v41\n\t" \
  "v_cvt_pk_bf16_f32 v63, v38, v39\n\t" \
  "v_exp_f32 v42, v42\n\t" \
  "v_exp_f32 v43, v43\n\t" \
  "v_cvt_pk_bf16_f32 v56, v40, v41\n\t" \
  "v_exp_f32 v44, v44\n\t" \
  "v_exp_f32 v45, v45\n\t" \
  "v_cvt_pk_bf16_f32 v57, v42, v43\n\t" \
  "v_mfma_f32_32x32x16_bf16 v[8:23], a[116:119], v[60:63], v[8:23]\n\t" \
  "v_exp_f32 v46, v46\n\t" \
  "v_exp_f32 v47, v47\n\t" \
  "v_cvt_pk_bf16_f32 v58, v44, v45\n\t" \
  "v_exp_f32 v48, v48\n\t" \
  "v_exp_f32 v49, v49\n\t" \
  "v_cvt_pk_bf16_f32 v59, v46, v47\n\t" \
  "v_mfma_f32_32x32x16_bf16 v[24:39], a[32:35], v[4:7], 0\n\t" \
  "v_exp_f32 v50, v50\n\t" \
  "v_exp_f32 v51, v51\n\t" \
  "v_cvt_pk_bf16_f32 v60, v48, v49\n\t" \
  "v_exp_f32 v52, v52\n\t" \
  "v_exp_f32 v53, v53\n\t" \
  "v_cvt_pk_bf16_f32 v61, v50, v51\n\t" \
  "v_mfma_f32_32x32x16_bf16 v[8:23], a[120:123], v[56:59], v[8:23]\n\t" \
  "v_exp_f32 v54, v54\n\t" \
  "v_exp_f32 v55, v55\n\t" \
  "v_cvt_pk_bf16_f32 v62, v52, v53\n\t" \
  "s_waitcnt vmcnt(14)\n\t" \
  "s_nop 0\n\t" \
  "global_load_dwordx4 a[56:59], v1, %[t0] offset:2048 sc1\n\t" \
  "s_mov_b64 exec, %[msk]\n\t" \
  "global_load_dwordx4 v[112:115], v1, %[t1] offset:2048 sc1\n\t" \
  "global_load_dwordx4 v[116:119], v1, %[t1] offset:2304 sc1\n\t" \
  "s_mov_b64 exec, -1\n\t" \
  "v_exp_f32 v24, v24\n\t" \
  "v_exp_f32 v25, v25\n\t" \
  "v_cvt_pk_bf16_f32 v63, v54, v55\n\t" \
  "v_exp_f32 v26, v26\n\t" \
  "v_exp_f32 v27, v27\n\t" \
  "v_cvt_pk_bf16_f32 v56, v24, v25\n\t" \
  "v_exp_f32 v28, v28\n\t" \
  "v_exp_f32 v29, v29\n\t" \
  "v_cvt_pk_bf16_f32 v57, v26, v27\n\t" \
  "v_mfma_f32_32x32x16_bf16 v[8:23], a[124:127], v[60:63], v[8:23]\n\t" \
  "v_exp_f32 v30, v30\n\t" \
  "v_exp_f32 v31, v31\n\t" \
  "v_cvt_pk_bf16_f32 v58, v28, v29\n\t" \
  "v_exp_f32 v32, v32\n\t" \
  "v_exp_f32 v33, v33\n\t" \
  "v_cvt_pk_bf16_f32 v59, v30, v31\n\t" \
  "v_mfma_f32_32x32x16_bf16 v[40:55], a[36:39], v[4:7], 0\n\t" \
  "v_exp_f32 v34, v34\n\t" \
  "v_exp_f32 v35, v35\n\t" \
  "v_cvt_pk_bf16_f32 v60, v32, v33\n\t" \
  "v_exp_f32 v36, v36\n\t" \
  "v_exp_f32 v37, v37\n\t" \
  "v_cvt_pk_bf16_f32 v61, v34, v35\n\t" \
  "v_mfma_f32_32x32x16_bf16 v[8:23], v[64:67], v[56:59], v[8:23]\n\t" \
  "v_exp_f32 v38, v38\n\t" \
  "v_exp_f32 v39, v39\n\t" \
  "v_cvt_pk_bf16_f32 v62, v36, v37\n\t" \
  "s_waitcnt vmcnt(14)\n\t" \
  "s_nop 0\n\t" \
  "global_load_dwordx4 a[60:63], v1, %[t0] offset:3072 sc1\n\t" \
  "s_mov_b64 exec, %[msk]\n\t" \
  "global_load_dwordx4 v[120:123], v1, %[t1] offset:3072 sc1\n\t" \
  "global_load_dwordx4 v[124:127], v1, %[t1] offset:3328 sc1\n\t" \
  "s_mov_b64 exec, -1\n\t" \
  "v_exp_f32 v40, v40\n\t" \
  "v_exp_f32 v41, v41\n\t" \
  "v_cvt_pk_bf16_f32 v63, v38, v39\n\t" \
  "v_exp_f32 v42, v42\n\t" \
  "v_exp_f32 v43, v43\n\t" \
  "v_cvt_pk_bf16_f32 v56, v40, v41\n\t" \
  "v_exp_f32 v44, v44\n\t" \
  "v_exp_f32 v45, v45\n\t" \
  "v_cvt_pk_bf16_f32 v57, v42, v43\n\t" \
  "v_mfma_f32_32x32x16_bf16 v[8:23], v[68:71], v[60:63], v[8:23]\n\t" \
  "v_exp_f32 v46, v46\n\t" \
  "v_exp_f32 v47, v47\n\t" \
  "v_cvt_pk_bf16_f32 v58, v44, v45\n\t" \
  "v_exp_f32 v48, v48\n\t" \
  "v_exp_f32 v49, v49\n\t" \
  "v_cvt_pk_bf16_f32 v59, v46, v47\n\t" \
  "v_mfma_f32_32x32x16_bf16 v[24:39], a[40:43], v[4:7], 0\n\t" \
  "v_exp_f32 v50, v50\n\t" \
  "v_exp_f32 v51, v51\n\t" \
  "v_cvt_pk_bf16_f32 v60, v48, v49\n\t" \
  "v_exp_f32 v52, v52\n\t" \
  "v_exp_f32 v53, v53\n\t" \
  "v_cvt_pk_bf16_f32 v61, v50, v51\n\t" \
  "v_mfma_f32_32x32x16_bf16 v[8:23], v[72:75], v[56:59], v[8:23]\n\t" \
  "v_exp_f32 v54, v54\n\t" \
  "v_exp_f32 v55, v55\n\t" \
  "v_cvt_pk_bf16_f32 v62, v52, v53\n\t" \
  "s_waitcnt vmcnt(14)\n\t" \
  "s_nop 0\n\t" \
  "v_exp_f32 v24, v24\n\t" \
  "v_exp_f32 v25, v25\n\t" \
  "v_cvt_pk_bf16_f32 v63, v54, v55\n\t" \
  "v_exp_f32 v26, v26\n\t" \
  "v_exp_f32 v27, v27\n\t" \
  "v_cvt_pk_bf16_f32 v56, v24, v25\n\t" \
  "v_exp_f32 v28, v28\n\t" \
  "v_exp_f32 v29, v29\n\t" \
  "v_cvt_pk_bf16_f32 v57, v26, v27\n\t" \
  "v_mfma_f32_32x32x16_bf16 v[8:23], v[76:79], v[60:63], v[8:23]\n\t" \
  "v_exp_f32 v30, v30\n\t" \
  "v_exp_f32 v31, v31\n\t" \
  "v_cvt_pk_bf16_f32 v58, v28, v29\n\t" \
  "v_exp_f32 v32, v32\n\t" \
  "v_exp_f32 v33, v33\n\t" \
  "v_cvt_pk_bf16_f32 v59, v30, v31\n\t" \
  "v_mfma_f32_32x32x16_bf16 v[40:55], a[44:47], v[4:7], 0\n\t" \
  "v_exp_f32 v34, v34\n\t" \
  "v_exp_f32 v35, v35\n\t" \
  "v_cvt_pk_bf16_f32 v60, v32, v33\n\t" \
  "v_exp_f32 v36, v36\n\t" \
  "v_exp_f32 v37, v37\n\t" \
  "v_cvt_pk_bf16_f32 v61, v34, v35\n\t" \
  "v_mfma_f32_32x32x16_bf16 v[8:23], v[80:83], v[56:59], v[8:23]\n\t" \
  "v_exp_f32 v38, v38\n\t" \
  "v_exp_f32 v39, v39\n\t" \
  "v_cvt_pk_bf16_f32 v62, v36, v37\n\t" \
  "s_waitcnt vmcnt(11)\n\t" \
  "s_nop 0\n\t" \
  "v_exp_f32 v40, v40\n\t" \
  "v_exp_f32 v41, v41\n\t" \
  "v_cvt_pk_bf16_f32 v63, v38, v39\n\t" \
  "v_exp_f32 v42, v42\n\t" \
  "v_exp_f32 v43, v43\n\t" \
  "v_cvt_pk_bf16_f32 v56, v40, v41\n\t" \
  "v_exp_f32 v44, v44\n\t" \
  "v_exp_f32 v45, v45\n\t" \
  "v_cvt_pk_bf16_f32 v57, v42, v43\n\t" \
  "v_mfma_f32_32x32x16_bf16 v[8:23], v[84:87], v[60:63], v[8:23]\n\t" \
  "v_exp_f32 v46, v46\n\t" \
  "v_exp_f32 v47, v47\n\t" \
  "v_cvt_pk_bf16_f32 v58, v44, v45\n\t" \
  "v_exp_f32 v48, v48\n\t" \
  "v_exp_f32 v49, v49\n\t" \
  "v_cvt_pk_bf16_f32 v59, v46, v47\n\t" \
  "v_mfma_f32_32x32x16_bf16 v[24:39], a[48:51], v[4:7], 0\n\t" \
  "v_exp_f32 v50, v50\n\t" \
  "v_exp_f32 v51, v51\n\t" \
  "v_cvt_pk_bf16_f32 v60, v48, v49\n\t" \
  "v_exp_f32 v52, v52\n\t" \
  "v_exp_f32 v53, v53\n\t" \
  "v_cvt_pk_bf16_f32 v61, v50, v51\n\t" \
  "v_mfma_f32_32x32x16_bf16 v[8:23], v[88:91], v[56:59], v[8:23]\n\t" \
  "v_exp_f32 v54, v54\n\t" \
  "v_exp_f32 v55, v55\n\t" \
  "v_cvt_pk_bf16_f32 v62, v52, v53\n\t" \
  "s_waitcnt vmcnt(8)\n\t" \
  "s_nop 0\n\t" \
  "v_exp_f32 v24, v24\n\t" \
  "v_exp_f32 v25, v25\n\t" \
  "v_cvt_pk_bf16_f32 v63, v54, v55\n\t" \
  "v_exp_f32 v26, v26\n\t" \
  "v_exp_f32 v27, v27\n\t" \
  "v_cvt_pk_bf16_f32 v56, v24, v25\n\t" \
  "v_exp_f32 v28, v28\n\t" \
  "v_exp_f32 v29, v29\n\t" \
  "v_cvt_pk_bf16_f32 v57, v26, v27\n\t" \
  "v_mfma_f32_32x32x16_bf16 v[8:23], v[92:95], v[60:63], v[8:23]\n\t" \
  "v_exp_f32 v30, v30\n\t" \
  "v_exp_f32 v31, v31\n\t" \
  "v_cvt_pk_bf16_f32 v58, v28, v29\n\t" \
  "v_exp_f32 v32, v32\n\t" \
  "v_exp_f32 v33, v33\n\t" \
  "v_cvt_pk_bf16_f32 v59, v30, v31\n\t" \
  "v_mfma_f32_32x32x16_bf16 v[40:55], a[52:55], v[4:7], 0\n\t" \
  "v_exp_f32 v34, v34\n\t" \
  "v_exp_f32 v35, v35\n\t" \
  "v_cvt_pk_bf16_f32 v60, v32, v33\n\t" \
  "v_exp_f32 v36, v36\n\t" \
  "v_exp_f32 v37, v37\n\t" \
  "v_cvt_pk_bf16_f32 v61, v34, v35\n\t" \
  "v_mfma_f32_32x32x16_bf16 v[8:23], v[96:99], v[56:59], v[8:23]\n\t" \
  "v_exp_f32 v38, v38\n\t" \
  "v_exp_f32 v39, v39\n\t" \
  "v_cvt_pk_bf16_f32 v62, v36, v37\n\t" \
  "s_waitcnt vmcnt(5)\n\t" \
  "s_nop 0\n\t" \
  "v_exp_f32 v40, v40\n\t" \
  "v_exp_f32 v41, v41\n\t" \
  "v_cvt_pk_bf16_f32 v63, v38, v39\n\t" \
  "v_exp_f32 v42, v42\n\t" \
  "v_exp_f32 v43, v43\n\t" \
  "v_cvt_pk_bf16_f32 v56, v40, v41\n\t" \
  "v_exp_f32 v44, v44\n\t" \
  "v_exp_f32 v45, v45\n\t" \
  "v_cvt_pk_bf16_f32 v57, v42, v43\n\t" \
  "v_mfma_f32_32x32x16_bf16 v[8:23], v[100:103], v[60:63], v[8:23]\n\t" \
  "v_exp_f32 v46, v46\n\t" \
  "v_exp_f32 v47, v47\n\t" \
  "v_cvt_pk_bf16_f32 v58, v44, v45\n\t" \
  "v_exp_f32 v48, v48\n\t" \
  "v_exp_f32 v49, v49\n\t" \
  "v_cvt_pk_bf16_f32 v59, v46, v47\n\t" \
  "v_mfma_f32_32x32x16_bf16 v[24:39], a[56:59], v[4:7], 0\n\t" \
  "v_exp_f32 v50, v50\n\t" \
  "v_exp_f32 v51, v51\n\t" \
  "v_cvt_pk_bf16_f32 v60, v48, v49\n\t" \
  "v_exp_f32 v52, v52\n\t" \
  "v_exp_f32 v53, v53\n\t" \
  "v_cvt_pk_bf16_f32 v61, v50, v51\n\t" \
  "v_mfma_f32_32x32x16_bf16 v[8:23], v[104:107], v[56:59], v[8:23]\n\t" \
  "v_exp_f32 v54, v54\n\t" \
  "v_exp_f32 v55, v55\n\t" \
  "v_cvt_pk_bf16_f32 v62, v52, v53\n\t" \
  "s_waitcnt vmcnt(2)\n\t" \
  "s_nop 0\n\t" \
  "v_exp_f32 v24, v24\n\t" \
  "v_exp_f32 v25, v25\n\t" \
  "v_cvt_pk_bf16_f32 v63, v54, v55\n\t" \
  "v_exp_f32 v26, v26\n\t" \
  "v_exp_f32 v27, v27\n\t" \
  "v_cvt_pk_bf16_f32 v56, v24, v25\n\t" \
  "v_exp_f32 v28, v28\n\t" \
  "v_exp_f32 v29, v29\n\t" \
  "v_cvt_pk_bf16_f32 v57, v26, v27\n\t" \
  "v_mfma_f32_32x32x16_bf16 v[8:23], v[108:111], v[60:63], v[8:23]\n\t" \
  "v_exp_f32 v30, v30\n\t" \
  "v_exp_f32 v31, v31\n\t" \
  "v_cvt_pk_bf16_f32 v58, v28, v29\n\t" \
  "v_exp_f32 v32, v32\n\t" \
  "v_exp_f32 v33, v33\n\t" \
  "v_cvt_pk_bf16_f32 v59, v30, v31\n\t" \
  "v_mfma_f32_32x32x16_bf16 v[40:55], a[60:63], v[4:7], 0\n\t" \
  "ds_read_b128 v[4:7], v2\n\t" \
  "v_add_u32 v2, 0x400, v2\n\t" \
  "v_exp_f32 v34, v34\n\t" \
  "v_exp_f32 v35, v35\n\t" \
  "v_cvt_pk_bf16_f32 v60, v32, v33\n\t" \
  "v_exp_f32 v36, v36\n\t" \
  "v_exp_f32 v37, v37\n\t" \
  "v_cvt_pk_bf16_f32 v61, v34, v35\n\t" \
  "v_mfma_f32_32x32x16_bf16 v[8:23], v[112:115], v[56:59], v[8:23]\n\t" \
  "v_exp_f32 v38, v38\n\t" \
  "v_exp_f32 v39, v39\n\t" \
  "v_cvt_pk_bf16_f32 v62, v36, v37\n\t" \
  "s_waitcnt vmcnt(0)\n\t" \
  "s_nop 0\n\t" \
  "v_exp_f32 v40, v40\n\t" \
  "v_exp_f32 v41, v41\n\t" \
  "v_cvt_pk_bf16_f32 v63, v38, v39\n\t" \
  "v_exp_f32 v42, v42\n\t" \
  "v_exp_f32 v43, v43\n\t" \
  "v_cvt_pk_bf16_f32 v56, v40, v41\n\t" \
  "v_exp_f32 v44, v44\n\t" \
  "v_exp_f32 v45, v45\n\t" \
  "v_cvt_pk_bf16_f32 v57, v42, v43\n\t" \
  "v_mfma_f32_32x32x16_bf16 v[8:23], v[116:119], v[60:63], v[8:23]\n\t" \
  "v_exp_f32 v46, v46\n\t" \
  "v_exp_f32 v47, v47\n\t" \
  "v_cvt_pk_bf16_f32 v58, v44, v45\n\t" \
  "v_exp_f32 v48, v48\n\t" \
  "v_exp_f32 v49, v49\n\t" \
  "v_cvt_pk_bf16_f32 v59, v46, v47\n\t" \
  "s_waitcnt lgkmcnt(0)\n\t" \
  "s_nop 0\n\t" \
  "v_mfma_f32_32x32x16_bf16 v[24:39], a[0:3], v[4:7], 0\n\t" \
  "v_exp_f32 v50, v50\n\t" \
  "v_exp_f32 v51, v51\n\t" \
  "v_cvt_pk_bf16_f32 v60, v48, v49\n\t" \
  "v_exp_f32 v52, v52\n\t" \
  "v_exp_f32 v53, v53\n\t" \
  "v_cvt_pk_bf16_f32 v61, v50, v51\n\t" \
  "v_mfma_f32_32x32x16_bf16 v[8:23], v[120:123], v[56:59], v[8:23]\n\t" \
  "v_exp_f32 v54, v54\n\t" \
  "v_exp_f32 v55, v55\n\t" \
  "v_cvt_pk_bf16_f32 v62, v52, v53\n\t" \
  "Lverd%=:\n\t" \
  "ds_read_b32 v1, v3 offset:160\n\t" \
  "s_waitcnt lgkmcnt(0)\n\t" \
  "v_readfirstlane_b32 %[ok], v1\n\t" \
  "s_nop 3\n\t" \
  "s_cmp_eq_u32 %[ok], 0\n\t" \
  "s_cbranch_scc1 Lverd%=\n\t" \
  "s_cmp_eq_u32 %[ok], 2\n\t" \
  "s_cbranch_scc1 Lexit%=\n\t" \
  "s_movk_i32 %[cnt], 7\n\t" \
  ".p2align 3\n\t" \
  "Lloop%=:\n\t" \
  "v_exp_f32 v24, v24\n\t" \
  "v_exp_f32 v25, v25\n\t" \
  "v_cvt_pk_bf16_f32 v63, v54, v55\n\t" \
  "v_exp_f32 v26, v26\n\t" \
  "v_exp_f32 v27, v27\n\t" \
  "v_cvt_pk_bf16_f32 v56, v24, v25\n\t" \
  "v_exp_f32 v28, v28\n\t" \
  "v_exp_f32 v29, v29\n\t" \
  "v_cvt_pk_bf16_f32 v57, v26, v27\n\t" \
  "v_mfma_f32_32x32x16_bf16 v[8:23], v[124:127], v[60:63], v[8:23]\n\t" \
  "v_exp_f32 v30, v30\n\t" \
  "v_exp_f32 v31, v31\n\t" \
  "v_cvt_pk_bf16_f32 v58, v28, v29\n\t" \
  "v_exp_f32 v32, v32\n\t" \
  "v_exp_f32 v33, v33\n\t" \
  "v_cvt_pk_bf16_f32 v59, v30, v31\n\t" \
  "v_mfma_f32_32x32x16_bf16 v[40:55], a[4:7], v[4:7], 0\n\t" \
  "v_exp_f32 v34, v34\n\t" \
  "v_exp_f32 v35, v35\n\t" \
  "v_cvt_pk_bf16_f32 v60, v32, v33\n\t" \
  "ds_write_b128 v0, v[8:11]\n\t" \
  "ds_write_b128 v0, v[12:15] offset:32\n\t" \
  "ds_write_b32 v0, v16 offset:64\n\t" \
  "ds_write_b32 v3, v3\n\t" \
  "v_add_u32 v0, %[pstep], v0\n\t" \
  "v_add_u32 v3, 16, v3\n\t" \
  "v_exp_f32 v36, v36\n\t" \
  "v_exp_f32 v37, v37\n\t" \
  "v_cvt_pk_bf16_f32 v61, v34, v35\n\t" \
  "v_mfma_f32_32x32x16_bf16 v[8:23], a[64:67], v[56:59], 0\n\t" \
  "v_exp_f32 v38, v38\n\t" \
  "v_exp_f32 v39, v39\n\t" \
  "v_cvt_pk_bf16_f32 v62, v36, v37\n\t" \
  "v_exp_f32 v40, v40\n\t" \
  "v_exp_f32 v41, v41\n\t" \
  "v_cvt_pk_bf16_f32 v63, v38, v39\n\t" \
  "v_exp_f32 v42, v42\n\t" \
  "v_exp_f32 v43, v43\n\t" \
  "v_cvt_pk_bf16_f32 v56, v40, v41\n\t" \
  "v_exp_f32 v44, v44\n\t" \
  "v_exp_f32 v45, v45\n\t" \
  "v_cvt_pk_bf16_f32 v57, v42, v43\n\t" \
  "v_mfma_f32_32x32x16_bf16 v[8:23], a[68:71], v[60:63], v[8:23]\n\t" \
  "v_exp_f32 v46, v46\n\t" \
  "v_exp_f32 v47, v47\n\t" \
  "v_cvt_pk_bf16_f32 v58, v44, v45\n\t" \
  "v_exp_f32 v48, v48\n\t" \
  "v_exp_f32 v49, v49\n\t" \
  "v_cvt_pk_bf16_f32 v59, v46, v47\n\t" \
  "v_mfma_f32_32x32x16_bf16 v[24:39], a[8:11], v[4:7], 0\n\t" \
  "v_exp_f32 v50, v50\n\t" \
  "v_exp_f32 v51, v51\n\t" \
  "v_cvt_pk_bf16_f32 v60, v48, v49\n\t" \
  "v_exp_f32 v52, v52\n\t" \
  "v_exp_f32 v53, v53\n\t" \
  "v_cvt_pk_bf16_f32 v61, v50, v51\n\t" \
  "v_mfma_f32_32x32x16_bf16 v[8:23], a[72:75], v[56:59], v[8:23]\n\t" \
  "v_exp_f32 v54, v54\n\t" \
  "v_exp_f32 v55, v55\n\t" \
  "v_cvt_pk_bf16_f32 v62, v52, v53\n\t" \
  "v_exp_f32 v24, v24\n\t" \
  "v_exp_f32 v25, v25\n\t" \
  "v_cvt_pk_bf16_f32 v63, v54, v55\n\t" \
  "v_exp_f32 v26, v26\n\t" \
  "v_exp_f32 v27, v27\n\t" \
  "v_cvt_pk_bf16_f32 v56, v24, v25\n\t" \
  "v_exp_f32 v28, v28\n\t" \
  "v_exp_f32 v29, v29\n\t" \
  "v_cvt_pk_bf16_f32 v57, v26, v27\n\t" \
  "v_mfma_f32_32x32x16_bf16 v[8:23], a[76:79], v[60:63], v[8:23]\n\t" \
  "v_exp_f32 v30, v30\n\t" \
  "v_exp_f32 v31, v31\n\t" \
  "v_cvt_pk_bf16_f32 v58, v28, v29\n\t" \
  "v_exp_f32 v32, v32\n\t" \
  "v_exp_f32 v33, v33\n\t" \
  "v_cvt_pk_bf16_f32 v59, v30, v31\n\t" \
  "v_mfma_f32_32x32x16_bf16 v[40:55], a[12:15], v[4:7], 0\n\t" \
  "v_exp_f32 v34, v34\n\t" \
  "v_exp_f32 v35, v35\n\t" \
  "v_cvt_pk_bf16_f32 v60, v32, v33\n\t" \
  "v_exp_f32 v36, v36\n\t" \
  "v_exp_f32 v37, v37\n\t" \
  "v_cvt_pk_bf16_f32 v61, v34, v35\n\t" \
  "v_mfma_f32_32x32x16_bf16 v[8:23], a[80:83], v[56:59], v[8:23]\n\t" \
  "v_exp_f32 v38, v38\n\t" \
  "v_exp_f32 v39, v39\n\t" \
  "v_cvt_pk_bf16_f32 v62, v36, v37\n\t" \
  "v_exp_f32 v40, v40\n\t" \
  "v_exp_f32 v41, v41\n\t" \
  "v_cvt_pk_bf16_f32 v63, v38, v39\n\t" \
  "v_exp_f32 v42, v42\n\t" \
  "v_exp_f32 v43, v43\n\t" \
  "v_cvt_pk_bf16_f32 v56, v40, v41\n\t" \
  "v_exp_f32 v44, v44\n\t" \
  "v_exp_f32 v45, v45\n\t" \
  "v_cvt_pk_bf16_f32 v57, v42, v43\n\t" \
  "v_mfma_f32_32x32x16_bf16 v[8:23], a[84:87], v[60:63], v[8:23]\n\t" \
  "v_exp_f32 v46, v46\n\t" \
  "v_exp_f32 v47, v47\n\t" \
  "v_cvt_pk_bf16_f32 v58, v44, v45\n\t" \
  "v_exp_f32 v48, v48\n\t" \
  "v_exp_f32 v49, v49\n\t" \
  "v_cvt_pk_bf16_f32 v59, v46, v47\n\t" \
  "v_mfma_f32_32x32x16_bf16 v[24:39], a[16:19], v[4:7], 0\n\t" \
  "v_exp_f32 v50, v50\n\t" \
  "v_exp_f32 v51, v51\n\t" \
  "v_cvt_pk_bf16_f32 v60, v48, v49\n\t" \
  "v_exp_f32 v52, v52\n\t" \
  "v_exp_f32 v53, v53\n\t" \
  "v_cvt_pk_bf16_f32 v61, v50, v51\n\t" \
  "v_mfma_f32_32x32x16_bf16 v[8:23], a[88:91], v[56:59], v[8:23]\n\t" \
  "v_exp_f32 v54, v54\n\t" \
  "v_exp_f32 v55, v55\n\t" \
  "v_cvt_pk_bf16_f32 v62, v52, v53\n\t" \
  "v_exp_f32 v24, v24\n\t" \
  "v_exp_f32 v25, v25\n\t" \
  "v_cvt_pk_bf16_f32 v63, v54, v55\n\t" \
  "v_exp_f32 v26, v26\n\t" \
  "v_exp_f32 v27, v27\n\t" \
  "v_cvt_pk_bf16_f32 v56, v24, v25\n\t" \
  "v_exp_f32 v28, v28\n\t" \
  "v_exp_f32 v29, v29\n\t" \
  "v_cvt_pk_bf16_f32 v57, v26, v27\n\t" \
  "v_mfma_f32_32x32x16_bf16 v[8:23], a[92:95], v[60:63], v[8:23]\n\t" \
  "v_exp_f32 v30, v30\n\t" \
  "v_exp_f32 v31, v31\n\t" \
  "v_cvt_pk_bf16_f32 v58, v28, v29\n\t" \
  "v_exp_f32 v32, v32\n\t" \
  "v_exp_f32 v33, v33\n\t" \
  "v_cvt_pk_bf16_f32 v59, v30, v31\n\t" \
  "v_mfma_f32_32x32x16_bf16 v[40:55], a[20:23], v[4:7], 0\n\t" \
  "v_exp_f32 v34, v34\n\t" \
  "v_exp_f32 v35, v35\n\t" \
  "v_cvt_pk_bf16_f32 v60, v32, v33\n\t" \
  "v_exp_f32 v36, v36\n\t" \
  "v_exp_f32 v37, v37\n\t" \
  "v_cvt_pk_bf16_f32 v61, v34, v35\n\t" \
  "v_mfma_f32_32x32x16_bf16 v[8:23], a[96:99], v[56:59], v[8:23]\n\t" \
  "v_exp_f32 v38, v38\n\t" \
  "v_exp_f32 v39, v39\n\t" \
  "v_cvt_pk_bf16_f32 v62, v36, v37\n\t" \
  "v_exp_f32 v40, v40\n\t" \
  "v_exp_f32 v41, v41\n\t" \
  "v_cvt_pk_bf16_f32 v63, v38, v39\n\t" \
  "v_exp_f32 v42, v42\n\t" \
  "v_exp_f32 v43, v43\n\t" \
  "v_cvt_pk_bf16_f32 v56, v40, v41\n\t" \
  "v_exp_f32 v44, v44\n\t" \
  "v_exp_f32 v45, v45\n\t" \
  "v_cvt_pk_bf16_f32 v57, v42, v43\n\t" \
  "v_mfma_f32_32x32x16_bf16 v[8:23], a[100:103], v[60:63], v[8:23]\n\t" \
  "v_exp_f32 v46, v46\n\t" \
  "v_exp_f32 v47, v47\n\t" \
  "v_cvt_pk_bf16_f32 v58, v44, v45\n\t" \
  "v_exp_f32 v48, v48\n\t" \
  "v_exp_f32 v49, v49\n\t" \
  "v_cvt_pk_bf16_f32 v59, v46, v47\n\t" \
  "v_mfma_f32_32x32x16_bf16 v[24:39], a[24:27], v[4:7], 0\n\t" \
  "v_exp_f32 v50, v50\n\t" \
  "v_exp_f32 v51, v51\n\t" \
  "v_cvt_pk_bf16_f32 v60, v48, v49\n\t" \
  "v_exp_f32 v52, v52\n\t" \
  "v_exp_f32 v53, v53\n\t" \
  "v_cvt_pk_bf16_f32 v61, v50, v51\n\t" \
  "v_mfma_f32_32x32x16_bf16 v[8:23], a[104:107], v[56:59], v[8:23]\n\t" \
  "v_exp_f32 v54, v54\n\t" \
  "v_exp_f32 v55, v55\n\t" \
  "v_cvt_pk_bf16_f32 v62, v52, v53\n\t" \
  "v_exp_f32 v24, v24\n\t" \
  "v_exp_f32 v25, v25\n\t" \
  "v_cvt_pk_bf16_f32 v63, v54, v55\n\t" \
  "v_exp_f32 v26, v26\n\t" \
  "v_exp_f32 v27, v27\n\t" \
  "v_cvt_pk_bf16_f32 v56, v24, v25\n\t" \
  "v_exp_f32 v28, v28\n\t" \
  "v_exp_f32 v29, v29\n\t" \
  "v_cvt_pk_bf16_f32 v57, v26, v27\n\t" \
  "v_mfma_f32_32x32x16_bf16 v[8:23], a[108:111], v[60:63], v[8:23]\n\t" \
  "v_exp_f32 v30, v30\n\t" \
  "v_exp_f32 v31, v31\n\t" \
  "v_cvt_pk_bf16_f32 v58, v28, v29\n\t" \
  "v_exp_f32 v32, v32\n\t" \
  "v_exp_f32 v33, v33\n\t" \
  "v_cvt_pk_bf16_f32 v59, v30, v31\n\t" \
  "v_mfma_f32_32x32x16_bf16 v[40:55], a[28:31], v[4:7], 0\n\t" \
  "v_exp_f32 v34, v34\n\t" \
  "v_exp_f32 v35, v35\n\t" \
  "v_cvt_pk_bf16_f32 v60, v32, v33\n\t" \
  "v_exp_f32 v36, v36\n\t" \
  "v_exp_f32 v37, v37\n\t" \
  "v_cvt_pk_bf16_f32 v61, v34, v35\n\t" \
  "v_mfma_f32_32x32x16_bf16 v[8:23], a[112:115], v[56:59], v[8:23]\n\t" \
  "v_exp_f32 v38, v38\n\t" \
  "v_exp_f32 v39, v39\n\t" \
  "v_cvt_pk_bf16_f32 v62, v36, v37\n\t" \
  "v_exp_f32 v40, v40\n\t" \
  "v_exp_f32 v41, v41\n\t" \
  "v_cvt_pk_bf16_f32 v63, v38, v39\n\t" \
  "v_exp_f32 v42, v42\n\t" \
  "v_exp_f32 v43, v43\n\t" \
  "v_cvt_pk_bf16_f32 v56, v40, v41\n\t" \
  "v_exp_f32 v44, v44\n\t" \
  "v_exp_f32 v45, v45\n\t" \
  "v_cvt_pk_bf16_f32 v57, v42, v43\n\t" \
  "v_mfma_f32_32x32x16_bf16 v[8:23], a[116:119], v[60:63], v[8:23]\n\t" \
  "v_exp_f32 v46, v46\n\t" \
  "v_exp_f32 v47, v47\n\t" \
  "v_cvt_pk_bf16_f32 v58, v44, v45\n\t" \
  "v_exp_f32 v48, v48\n\t" \
  "v_exp_f32 v49, v49\n\t" \
  "v_cvt_pk_bf16_f32 v59, v46, v47\n\t" \
  "v_mfma_f32_32x32x16_bf16 v[24:39], a[32:35], v[4:7], 0\n\t" \
  "v_exp_f32 v50, v50\n\t" \
  "v_exp_f32 v51, v51\n\t" \
  "v_cvt_pk_bf16_f32 v60, v48, v49\n\t" \
  "v_exp_f32 v52, v52\n\t" \
  "v_exp_f32 v53, v53\n\t" \
  "v_cvt_pk_bf16_f32 v61, v50, v51\n\t" \
  "v_mfma_f32_32x32x16_bf16 v[8:23], a[120:123], v[56:59], v[8:23]\n\t" \
  "v_exp_f32 v54, v54\n\t" \
  "v_exp_f32 v55, v55\n\t" \
  "v_cvt_pk_bf16_f32 v62, v52, v53\n\t" \
  "v_exp_f32 v24, v24\n\t" \
  "v_exp_f32 v25, v25\n\t" \
  "v_cvt_pk_bf16_f32 v63, v54, v55\n\t" \
  "v_exp_f32 v26, v26\n\t" \
  "v_exp_f32 v27, v27\n\t" \
  "v_cvt_pk_bf16_f32 v56, v24, v25\n\t" \
  "v_exp_f32 v28, v28\n\t" \
  "v_exp_f32 v29, v29\n\t" \
  "v_cvt_pk_bf16_f32 v57, v26, v27\n\t" \
  "v_mfma_f32_32x32x16_bf16 v[8:23], a[124:127], v[60:63], v[8:23]\n\t" \
  "v_exp_f32 v30, v30\n\t" \
  "v_exp_f32 v31, v31\n\t" \
  "v_cvt_pk_bf16_f32 v58, v28, v29\n\t" \
  "v_exp_f32 v32, v32\n\t" \
  "v_exp_f32 v33, v33\n\t" \
  "v_cvt_pk_bf16_f32 v59, v30, v31\n\t" \
  "v_mfma_f32_32x32x16_bf16 v[40:55], a[36:39], v[4:7], 0\n\t" \
  "v_exp_f32 v34, v34\n\t" \
  "v_exp_f32 v35, v35\n\t" \
  "v_cvt_pk_bf16_f32 v60, v32, v33\n\t" \
  "v_exp_f32 v36, v36\n\t" \
  "v_exp_f32 v37, v37\n\t" \
  "v_cvt_pk_bf16_f32 v61, v34, v35\n\t" \
  "v_mfma_f32_32x32x16_bf16 v[8:23], v[64:67], v[56:59], v[8:23]\n\t" \
  "v_exp_f32 v38, v38\n\t" \
  "v_exp_f32 v39, v39\n\t" \
  "v_cvt_pk_bf16_f32 v62, v36, v37\n\t" \
  "v_exp_f32 v40, v40\n\t" \
  "v_exp_f32 v41, v41\n\t" \
  "v_cvt_pk_bf16_f32 v63, v38, v39\n\t" \
  "v_exp_f32 v42, v42\n\t" \
  "v_exp_f32 v43, v43\n\t" \
  "v_cvt_pk_bf16_f32 v56, v40, v41\n\t" \
  "v_exp_f32 v44, v44\n\t" \
  "v_exp_f32 v45, v45\n\t" \
  "v_cvt_pk_bf16_f32 v57, v42, v43\n\t" \
  "v_mfma_f32_32x32x16_bf16 v[8:23], v[68:71], v[60:63], v[8:23]\n\t" \
  "v_exp_f32 v46, v46\n\t" \
  "v_exp_f32 v47, v47\n\t" \
  "v_cvt_pk_bf16_f32 v58, v44, v45\n\t" \
  "v_exp_f32 v48, v48\n\t" \
  "v_exp_f32 v49, v49\n\t" \
  "v_cvt_pk_bf16_f32 v59, v46, v47\n\t" \
  "v_mfma_f32_32x32x16_bf16 v[24:39], a[40:43], v[4:7], 0\n\t" \
  "v_exp_f32 v50, v50\n\t" \
  "v_exp_f32 v51, v51\n\t" \
  "v_cvt_pk_bf16_f32 v60, v48, v49\n\t" \
  "v_exp_f32 v52, v52\n\t" \
  "v_exp_f32 v53, v53\n\t" \
  "v_cvt_pk_bf16_f32 v61, v50, v51\n\t" \
  "v_mfma_f32_32x32x16_bf16 v[8:23], v[72:75], v[56:59], v[8:23]\n\t" \
  "v_exp_f32 v54, v54\n\t" \
  "v_exp_f32 v55, v55\n\t" \
  "v_cvt_pk_bf16_f32 v62, v52, v53\n\t" \
  "v_exp_f32 v24, v24\n\t" \
  "v_exp_f32 v25, v25\n\t" \
  "v_cvt_pk_bf16_f32 v63, v54, v55\n\t" \
  "v_exp_f32 v26, v26\n\t" \
  "v_exp_f32 v27, v27\n\t" \
  "v_cvt_pk_bf16_f32 v56, v24, v25\n\t" \
  "v_exp_f32 v28, v28\n\t" \
  "v_exp_f32 v29, v29\n\t" \
  "v_cvt_pk_bf16_f32 v57, v26, v27\n\t" \
  "v_mfma_f32_32x32x16_bf16 v[8:23], v[76:79], v[60:63], v[8:23]\n\t" \
  "v_exp_f32 v30, v30\n\t" \
  "v_exp_f32 v31, v31\n\t" \
  "v_cvt_pk_bf16_f32 v58, v28, v29\n\t" \
  "v_exp_f32 v32, v32\n\t" \
  "v_exp_f32 v33, v33\n\t" \
  "v_cvt_pk_bf16_f32 v59, v30, v31\n\t" \
  "v_mfma_f32_32x32x16_bf16 v[40:55], a[44:47], v[4:7], 0\n\t" \
  "v_exp_f32 v34, v34\n\t" \
  "v_exp_f32 v35, v35\n\t" \
  "v_cvt_pk_bf16_f32 v60, v32, v33\n\t" \
  "v_exp_f32 v36, v36\n\t" \
  "v_exp_f32 v37, v37\n\t" \
  "v_cvt_pk_bf16_f32 v61, v34, v35\n\t" \
  "v_mfma_f32_32x32x16_bf16 v[8:23], v[80:83], v[56:59], v[8:23]\n\t" \
  "v_exp_f32 v38, v38\n\t" \
  "v_exp_f32 v39, v39\n\t" \
  "v_cvt_pk_bf16_f32 v62, v36, v37\n\t" \
  "v_exp_f32 v40, v40\n\t" \
  "v_exp_f32 v41, v41\n\t" \
  "v_cvt_pk_bf16_f32 v63, v38, v39\n\t" \
  "v_exp_f32 v42, v42\n\t" \
  "v_exp_f32 v43, v43\n\t" \
  "v_cvt_pk_bf16_f32 v56, v40, v41\n\t" \
  "v_exp_f32 v44, v44\n\t" \
  "v_exp_f32 v45, v45\n\t" \
  "v_cvt_pk_bf16_f32 v57, v42, v43\n\t" \
  "v_mfma_f32_32x32x16_bf16 v[8:23], v[84:87], v[60:63], v[8:23]\n\t" \
  "v_exp_f32 v46, v46\n\t" \
  "v_exp_f32 v47, v47\n\t" \
  "v_cvt_pk_bf16_f32 v58, v44, v45\n\t" \
  "v_exp_f32 v48, v48\n\t" \
  "v_exp_f32 v49, v49\n\t" \
  "v_cvt_pk_bf16_f32 v59, v46, v47\n\t" \
  "v_mfma_f32_32x32x16_bf16 v[24:39], a[48:51], v[4:7], 0\n\t" \
  "v_exp_f32 v50, v50\n\t" \
  "v_exp_f32 v51, v51\n\t" \
  "v_cvt_pk_bf16_f32 v60, v48, v49\n\t" \
  "v_exp_f32 v52, v52\n\t" \
  "v_exp_f32 v53, v53\n\t" \
  "v_cvt_pk_bf16_f32 v61, v50, v51\n\t" \
  "v_mfma_f32_32x32x16_bf16 v[8:23], v[88:91], v[56:59], v[8:23]\n\t" \
  "v_exp_f32 v54, v54\n\t" \
  "v_exp_f32 v55, v55\n\t" \
  "v_cvt_pk_bf16_f32 v62, v52, v53\n\t" \
  "v_exp_f32 v24, v24\n\t" \
  "v_exp_f32 v25, v25\n\t" \
  "v_cvt_pk_bf16_f32 v63, v54, v55\n\t" \
  "v_exp_f32 v26, v26\n\t" \
  "v_exp_f32 v27, v27\n\t" \
  "v_cvt_pk_bf16_f32 v56, v24, v25\n\t" \
  "v_exp_f32 v28, v28\n\t" \
  "v_exp_f32 v29, v29\n\t" \
  "v_cvt_pk_bf16_f32 v57, v26, v27\n\t" \
  "v_mfma_f32_32x32x16_bf16 v[8:23], v[92:95], v[60:63], v[8:23]\n\t" \
  "v_exp_f32 v30, v30\n\t" \
  "v_exp_f32 v31, v31\n\t" \
  "v_cvt_pk_bf16_f32 v58, v28, v29\n\t" \
  "v_exp_f32 v32, v32\n\t" \
  "v_exp_f32 v33, v33\n\t" \
  "v_cvt_pk_bf16_f32 v59, v30, v31\n\t" \
  "v_mfma_f32_32x32x16_bf16 v[40:55], a[52:55], v[4:7], 0\n\t" \
  "v_exp_f32 v34, v34\n\t" \
  "v_exp_f32 v35, v35\n\t" \
  "v_cvt_pk_bf16_f32 v60, v32, v33\n\t" \
  "v_exp_f32 v36, v36\n\t" \
  "v_exp_f32 v37, v37\n\t" \
  "v_cvt_pk_bf16_f32 v61, v34, v35\n\t" \
  "v_mfma_f32_32x32x16_bf16 v[8:23], v[96:99], v[56:59], v[8:23]\n\t" \
  "v_exp_f32 v38, v38\n\t" \
  "v_exp_f32 v39, v39\n\t" \
  "v_cvt_pk_bf16_f32 v62, v36, v37\n\t" \
  "v_exp_f32 v40, v40\n\t" \
  "v_exp_f32 v41, v41\n\t" \
  "v_cvt_pk_bf16_f32 v63, v38, v39\n\t" \
  "v_exp_f32 v42, v42\n\t" \
  "v_exp_f32 v43, v43\n\t" \
  "v_cvt_pk_bf16_f32 v56, v40, v41\n\t" \
  "v_exp_f32 v44, v44\n\t" \
  "v_exp_f32 v45, v45\n\t" \
  "v_cvt_pk_bf16_f32 v57, v42, v43\n\t" \
  "v_mfma_f32_32x32x16_bf16 v[8:23], v[100:103], v[60:63], v[8:23]\n\t" \
  "v_exp_f32 v46, v46\n\t" \
  "v_exp_f32 v47, v47\n\t" \
  "v_cvt_pk_bf16_f32 v58, v44, v45\n\t" \
  "v_exp_f32 v48, v48\n\t" \
  "v_exp_f32 v49, v49\n\t" \
  "v_cvt_pk_bf16_f32 v59, v46, v47\n\t" \
  "v_mfma_f32_32x32x16_bf16 v[24:39], a[56:59], v[4:7], 0\n\t" \
  "v_exp_f32 v50, v50\n\t" \
  "v_exp_f32 v51, v51\n\t" \
  "v_cvt_pk_bf16_f32 v60, v48, v49\n\t" \
  "v_exp_f32 v52, v52\n\t" \
  "v_exp_f32 v53, v53\n\t" \
  "v_cvt_pk_bf16_f32 v61, v50, v51\n\t" \
  "v_mfma_f32_32x32x16_bf16 v[8:23], v[104:107], v[56:59], v[8:23]\n\t" \
  "v_exp_f32 v54, v54\n\t" \
  "v_exp_f32 v55, v55\n\t" \
  "v_cvt_pk_bf16_f32 v62, v52, v53\n\t" \
  "v_exp_f32 v24, v24\n\t" \
  "v_exp_f32 v25, v25\n\t" \
  "v_cvt_pk_bf16_f32 v63, v54, v55\n\t" \
  "v_exp_f32 v26, v26\n\t" \
  "v_exp_f32 v27, v27\n\t" \
  "v_cvt_pk_bf16_f32 v56, v24, v25\n\t" \
  "v_exp_f32 v28, v28\n\t" \
  "v_exp_f32 v29, v29\n\t" \
  "v_cvt_pk_bf16_f32 v57, v26, v27\n\t" \
  "v_mfma_f32_32x32x16_bf16 v[8:23], v[108:111], v[60:63], v[8:23]\n\t" \
  "v_exp_f32 v30, v30\n\t" \
  "v_exp_f32 v31, v31\n\t" \
  "v_cvt_pk_bf16_f32 v58, v28, v29\n\t" \
  "v_exp_f32 v32, v32\n\t" \
  "v_exp_f32 v33, v33\n\t" \
  "v_cvt_pk_bf16_f32 v59, v30, v31\n\t" \
  "v_mfma_f32_32x32x16_bf16 v[40:55], a[60:63], v[4:7], 0\n\t" \
  "ds_read_b128 v[4:7], v2\n\t" \
  "v_add_u32 v2, 0x400, v2\n\t" \
  "v_exp_f32 v34, v34\n\t" \
  "v_exp_f32 v35, v35\n\t" \
  "v_cvt_pk_bf16_f32 v60, v32, v33\n\t" \
  "v_exp_f32 v36, v36\n\t" \
  "v_exp_f32 v37, v37\n\t" \
  "v_cvt_pk_bf16_f32 v61, v34, v35\n\t" \
  "v_mfma_f32_32x32x16_bf16 v[8:23], v[112:115], v[56:59], v[8:23]\n\t" \
  "v_exp_f32 v38, v38\n\t" \
  "v_exp_f32 v39, v39\n\t" \
  "v_cvt_pk_bf16_f32 v62, v36, v37\n\t" \
  "v_exp_f32 v40, v40\n\t" \
  "v_exp_f32 v41, v41\n\t" \
  "v_cvt_pk_bf16_f32 v63, v38, v39\n\t" \
  "v_exp_f32 v42, v42\n\t" \
  "v_exp_f32 v43, v43\n\t" \
  "v_cvt_pk_bf16_f32 v56, v40, v41\n\t" \
  "v_exp_f32 v44, v44\n\t" \
  "v_exp_f32 v45, v45\n\t" \
  "v_cvt_pk_bf16_f32 v57, v42, v43\n\t" \
  "v_mfma_f32_32x32x16_bf16 v[8:23], v[116:119], v[60:63], v[8:23]\n\t" \
  "v_exp_f32 v46, v46\n\t" \
  "v_exp_f32 v47, v47\n\t" \
  "v_cvt_pk_bf16_f32 v58, v44, v45\n\t" \
  "v_exp_f32 v48, v48\n\t" \
  "v_exp_f32 v49, v49\n\t" \
  "v_cvt_pk_bf16_f32 v59, v46, v47\n\t" \
  "s_waitcnt lgkmcnt(0)\n\t" \
  "s_nop 0\n\t" \
  "v_mfma_f32_32x32x16_bf16 v[24:39], a[0:3], v[4:7], 0\n\t" \
  "v_exp_f32 v50, v50\n\t" \
  "v_exp_f32 v51, v51\n\t" \
  "v_cvt_pk_bf16_f32 v60, v48, v49\n\t" \
  "v_exp_f32 v52, v52\n\t" \
  "v_exp_f32 v53, v53\n\t" \
  "v_cvt_pk_bf16_f32 v61, v50, v51\n\t" \
  "v_mfma_f32_32x32x16_bf16 v[8:23], v[120:123], v[56:59], v[8:23]\n\t" \
  "v_exp_f32 v54, v54\n\t" \
  "v_exp_f32 v55, v55\n\t" \
  "v_cvt_pk_bf16_f32 v62, v52, v53\n\t" \
  "s_sub_u32 %[cnt], %[cnt], 1\n\t" \
  "s_cmp_lg_u32 %[cnt], 0\n\t" \
  "s_cbranch_scc1 Lloop%=\n\t" \
  "v_cvt_pk_bf16_f32 v63, v54, v55\n\t" \
  "s_nop 1\n\t" \
  "v_mfma_f32_32x32x16_bf16 v[8:23], v[124:127], v[60:63], v[8:23]\n\t" \
  "s_nop 7\n\t" \
  "s_nop 5\n\t" \
  "ds_write_b128 v0, v[8:11]\n\t" \
  "ds_write_b128 v0, v[12:15] offset:32\n\t" \
  "ds_write_b32 v0, v16 offset:64\n\t" \
  "ds_write_b32 v3, v3\n\t" \
  "v_add_u32 v0, %[pstep], v0\n\t" \
  "v_add_u32 v3, 16, v3\n\t" \
  "Lexit%=:\n\t" \
  "s_waitcnt vmcnt(0) lgkmcnt(0)\n\t"
#define ATTN_WAVE_CLOB "v4", "v5", "v6", "v7", "v8", "v9", "v10", "v11", "v12", "v13", "v14", "v15", "v16", "v17", "v18", "v19", "v20", "v21", "v22", "v23", "v27", "v28", "v29", "v30", "v31", "v32", "v33", "v34", "v35", "v36", "v37", "v38", "v39", "v40", "v41", "v42", "v43", "v44", "v45", "v46", "v47", "v48", "v49", "v50", "v51", "v52", "v53", "v54", "v55", "v56", "v57", "v58", "v59", "v60", "v61", "v62", "v63", "v64", "v65", "v66", "v67", "v68", "v69", "v70", "v71", "v72", "v73", "v74", "v75", "v76", "v77", "v78", "v79", "v80", "v81", "v82", "v83", "v84", "v85", "v86", "v87", "v88", "v89", "v90", "v91", "v92", "v93", "v94", "v95", "v96", "v97", "v98", "v99", "v100", "v101", "v102", "v103", "v104", "v105", "v106", "v107", "v108", "v109", "v110", "v111", "v112", "v113", "v114", "v115", "v116", "v117", "v118", "v119", "v120", "v121", "v122", "v123", "v124", "v125", "v126", "v127", "a0", "a1", "a2", "a3", "a4", "a5", "a6", "a7", "a8", "a9", "a10", "a11", "a12", "a13", "a14", "a15", "a16", "a17", "a18", "a19", "a20", "a21", "a22", "a23", "a24", "a25", "a26", "a27", "a28", "a29", "a30", "a31", "a32", "a33", "a34", "a35", "a36", "a37", "a38", "a39", "a40", "a41", "a42", "a43", "a44", "a45", "a46", "a47", "a48", "a49", "a50", "a51", "a52", "a53", "a54", "a55", "a56", "a57", "a58", "a59", "a60", "a61", "a62", "a63", "a64", "a65", "a66", "a67", "a68", "a69", "a70", "a71", "a72", "a73", "a74", "a75", "a76", "a77", "a78", "a79", "a80", "a81", "a82", "a83", "a84", "a85", "a86", "a87", "a88", "a89", "a90", "a91", "a92", "a93", "a94", "a95", "a96", "a97", "a98", "a99", "a100", "a101", "a102", "a103", "a104", "a105", "a106", "a107", "a108", "a109", "a110", "a111", "a112", "a113", "a114", "a115", "a116", "a117", "a118", "a119", "a120", "a121", "a122", "a123", "a124", "a125", "a126", "a127"

DI void k2_reduce(char* smem, char* ws, int b, int t, int lane, u64 tag) {
  lds_vu32* fl = (lds_vu32*)LDSP(smem + K2_OFF_FLAG);
  while (__builtin_amdgcn_ballot_w64(fl[t * 4 + (lane & 3)] == 0u) != 0ull) { if (t < 5) __builtin_amdgcn_s_sleep(8); else __builtin_amdgcn_s_sleep(1); }
  asm volatile("" ::: "memory");
  const int tok = lane & 7, gg = (lane >> 3) & 3, ksp = lane >> 5;
  const int cc = (2 * ksp + (gg >> 1)) * 8 + tok;
  const char* pp = smem + K2_OFF_PART + t * 4 * PART_STRIDE + cc * PROW;
  f32x4 a[4], bb[4];
  float l[4];
#pragma unroll
  for (int i = 0; i < 4; ++i) {
    l[i] = *(const float*)(pp + i * PART_STRIDE + 64);
    a[i] = *(const f32x4*)(pp + i * PART_STRIDE + (gg & 1) * 32);
    bb[i] = *(const f32x4*)(pp + i * PART_STRIDE + (gg & 1) * 32 + 16);
  }
  const float rl = __builtin_amdgcn_rcpf((l[0] + l[1]) + (l[2] + l[3]));
  const f32x4 sa = (a[0] + a[1]) + (a[2] + a[3]), sb = (bb[0] + bb[1]) + (bb[2] + bb[3]);
  u32x4 ov = {pack2(sa[0] * rl, sa[1] * rl), pack2(sa[2] * rl, sa[3] * rl), pack2(sb[0] * rl, sb[1] * rl), pack2(sb[2] * rl, sb[3] * rl)};
  if (t == 7) {
    *(u32x4*)(smem + K2_OFF_OBUF + t * 1024 + lane * 16) = ov;
    asm volatile("s_waitcnt lgkmcnt(0)" ::: "memory");
    fl[32 + t] = 1u;
  } else {
    if (lane == 0) lds_inc(smem + K2_OFF_FLAG + 208);
    const int owner = (b & ~7) | ((b + 1 + t) & 7);
    st_dev(ws_rsrc(ws), (unsigned)(lane * 16), (unsigned)WS_OX + (unsigned)((owner * 8 + (b & 7)) * 1024), ov);
    asm volatile("s_waitcnt vmcnt(0)" ::: "memory");
    if (lane == 0) st_tag(ws + WS_FOX + owner * 128 + (b & 7) * 8, tag);
  }
}
DI void k2_receive(char* smem, char* ws, int b, int t, int lane, u64 tag) {
  lds_vu32* fl = (lds_vu32*)LDSP(smem + K2_OFF_FLAG);
  const int src = (b - 1 - t) & 7;
  wait_tags<1, 2>(ws + WS_FOX + b * 128 + src * 8, true, tag);
  const u32x4 v = ld_dev(ws_rsrc(ws), (unsigned)(lane * 16), (unsigned)WS_OX + (unsigned)((b * 8 + src) * 1024));
  *(u32x4*)(smem + K2_OFF_OBUF + t * 1024 + lane * 16) = v;
  asm volatile("s_waitcnt lgkmcnt(0)" ::: "memory");
  fl[32 + t] = 1u;
}
DI void dma16(const char* src, const char* lds_dst) {
  unsigned keep;
  const unsigned dst = __builtin_amdgcn_readfirstlane((unsigned)(size_t)LDSP(lds_dst));
  asm volatile("s_mov_b32 %0, m0\n\ts_mov_b32 m0, %2\n\ts_nop 0\n\tglobal_load_lds_dwordx4 %1, off\n\ts_mov_b32 m0, %0"
               : "=&s"(keep) : "v"(src), "s"(dst) : "memory");
}

DI void k2_attn_slow(char* smem, const char* __restrict__ ws, int w) {
  const int lane = fresh_lane(), c = lane & 31, hh = lane >> 5, i16 = lane & 15;
  const f32x16 zero = {0.f, 0.f, 0.f, 0.f, 0.f, 0.f, 0.f, 0.f, 0.f, 0.f, 0.f, 0.f, 0.f, 0.f, 0.f, 0.f};
  const __amdgpu_buffer_rsrc_t rs = ws_rsrc((char*)ws);
  const unsigned kp = (unsigned)WS_K + lane * 16, vp = (unsigned)WS_VF + hh * 512 + i16 * 16;
  const unsigned cst = (c == 16) ? 0x3f803f80u : 0u;
  lds_vu32* fl = (lds_vu32*)LDSP(smem + K2_OFF_FLAG);
  for (int t = 0; t < 8; ++t) {
    const u32x4 qf = *(const u32x4*)(smem + K2_OFF_Q + t * 1024 + lane * 16);
    float mx = -INFINITY;
    for (int kt = 0; kt < 64; ++kt) {
      const f32x16 s = mfma32(ld_dev(rs, kp, kt * 1024), qf, zero);
#pragma unroll
      for (int r = 0; r < 16; ++r) mx = fmaxf(mx, s[r]);
    }
    mx = fmaxf(mx, shx(mx, 32));
    f32x16 o = zero;
    for (int kt = 16 * w; kt < 16 * w + 16; ++kt) {
      const f32x16 s = mfma32(ld_dev(rs, kp, kt * 1024), qf, zero);
      u32x4 v0 = ld_dev(rs, vp, kt * 1024), v1 = ld_dev(rs, vp, kt * 1024 + 256);
      if (c >= 16) { v0 = (u32x4){cst, cst, cst, cst}; v1 = v0; }
      float p[16];
#pragma unroll
      for (int r = 0; r < 16; ++r) p[r] = __builtin_amdgcn_exp2f(s[r] - mx);
      const u32x4 p0 = {pack2(p[0], p[1]), pack2(p[2], p[3]), pack2(p[4], p[5]), pack2(p[6], p[7])};
      const u32x4 p1 = {pack2(p[8], p[9]), pack2(p[10], p[11]), pack2(p[12], p[13]), pack2(p[14], p[15])};
      o = mfma32(v0, p0, o);
      o = mfma32(v1, p1, o);
    }
    char* pp = smem + K2_OFF_PART + (t * 4 + w) * PART_STRIDE + c * PROW;
    *(f32x4*)(pp + 16 * hh) = (f32x4){o[0], o[1], o[2], o[3]};
    *(f32x4*)(pp + 32 + 16 * hh) = (f32x4){o[4], o[5], o[6], o[7]};
    if (hh == 0) *(float*)(pp + 64) = o[8];
    asm volatile("s_waitcnt lgkmcnt(0)" ::: "memory");
    fl[t * 4 + w] = 1u;
  }
}

DI void dma16_dev(const char* src, const char* lds_dst) {
  unsigned keep;
  const unsigned dst = __builtin_amdgcn_readfirstlane((unsigned)(size_t)LDSP(lds_dst));
  asm volatile("s_mov_b32 %0, m0\n\ts_mov_b32 m0, %2\n\ts_nop 0\n\tglobal_load_lds_dwordx4 %1, off sc1\n\ts_mov_b32 m0, %0"
               : "=&s"(keep) : "v"(src), "s"(dst) : "memory");
}
constexpr int K4_ROW = 68 * 4;
constexpr int KM_LDS = K1_LDS;
static_assert(KM_LDS >= K2_LDS && KM_LDS >= 8 * 64 * K4_ROW && KM_LDS <= 163840, "LDS");

DI unsigned wgu_off(int idx, int kh, int cw, int k) {
  const int h0 = min(((idx & 15) * 4 + cw) * 3, NHU - 3);
  return (unsigned)WS_WGU + (unsigned)((kh * 8 + k) * NHU + h0) * 1024u;
}
DI void ffn_phases(char* smem, char* __restrict__ ws, u16* __restrict__ out, int w, int xcd, int idx, u32x4 (&wf)[8][3], u64 tag) {
  const int lane = fresh_lane();
  const unsigned l16 = (unsigned)lane * 16u;
  const int i16 = lane & 15, g = lane >> 4;
  const __amdgpu_buffer_rsrc_t rs = ws_rsrc(ws);
  const int mt = xcd * 2 + (idx >> 4), cg = idx & 15;
  const int kh = w >> 2, cw = w & 3;
  const int slot = cg * 4 + cw;
  const int h0 = min(slot * 3, NHU - 3);
  f32x4 acc[8][3];
#pragma unroll
  for (int m = 0; m < 8; ++m)
#pragma unroll
    for (int u = 0; u < 3; ++u) acc[m][u] = (f32x4){0.f, 0.f, 0.f, 0.f};
  const int nb = idx & 7, mt4 = xcd * 4 + (idx >> 3);
  const int k0 = w * 5 + min(w, 3);
  u32x4 wd4[6][4];
#define WD_LOAD(q_) wd4[(q_) >> 2][(q_) & 3] = ld_dev(rs, l16, (unsigned)WS_WD + (unsigned)((nb * 4 + ((q_) & 3)) * KS4 + k0 + ((q_) >> 2)) * 1024u)
  {
    if (w == 4) wait_tags<1, 1>(ws + WS_FH + (mt * 16 + i16) * 8, true, tag);
    __builtin_amdgcn_s_waitcnt(0x0F70);
    asm volatile("s_barrier" : "+a"(wf[6][0]), "+a"(wf[6][1]), "+a"(wf[6][2]), "+a"(wf[7][0]), "+a"(wf[7][1]), "+a"(wf[7][2]) :: "memory");
    {
      const char* hn = ws + WS_HN + (size_t)(mt * 8 + w) * 1024 + l16;
#pragma unroll
      for (int j = 0; j < 8; ++j) {
        dma16_dev(hn + (size_t)j * 131072, smem + (j * 8 + w) * 1024);
        dma16_dev(hn + (size_t)(8 + j) * 131072, smem + ((8 + j) * 8 + w) * 1024);
      }
    }
    const char* rd = smem + kh * 65536 + (1 - kh) * 4096 + l16;
    const char* rd1 = smem + kh * 65536 + kh * 4096 + l16;
    u32x4 af[2][4];
    asm volatile("s_waitcnt vmcnt(14)\n\ts_barrier" ::: "memory");
#pragma unroll
    for (int m = 0; m < 4; ++m) af[0][m] = *(const u32x4*)(rd + m * 1024);
#pragma unroll
    for (int j = 0; j < 8; ++j) {
      __builtin_amdgcn_sched_barrier(0);
#pragma unroll
      for (int m = 0; m < 4; ++m) af[1][m] = *(const u32x4*)(rd1 + j * 8192 + m * 1024);
#pragma unroll
      for (int m = 0; m < 4; ++m)
#pragma unroll
        for (int u = 0; u < 3; ++u) acc[m][u] = mfma16(wf[j][u], af[0][m], acc[m][u]);
#pragma unroll
      for (int i = 0; i < 4; ++i) { __builtin_amdgcn_sched_group_barrier(0x8, 1, 0); __builtin_amdgcn_sched_group_barrier(0x100, 1, 0); }
      __builtin_amdgcn_sched_group_barrier(0x8, 8, 0);
      __builtin_amdgcn_sched_barrier(0);
      if (j < 7) {
        if (j == 0) asm volatile("s_waitcnt vmcnt(12)\n\ts_barrier" ::: "memory");
        if (j == 1) asm volatile("s_waitcnt vmcnt(10)\n\ts_barrier" ::: "memory");
        if (j == 2) asm volatile("s_waitcnt vmcnt(8)\n\ts_barrier" ::: "memory");
        if (j == 3) asm volatile("s_waitcnt vmcnt(9)\n\ts_barrier" ::: "memory");
        if (j == 4) asm volatile("s_waitcnt vmcnt(10)\n\ts_barrier" ::: "memory");
        if (j == 5) asm volatile("s_waitcnt vmcnt(11)\n\ts_barrier" ::: "memory");
        if (j == 6) asm volatile("s_waitcnt vmcnt(12)\n\ts_barrier" ::: "memory");
#pragma unroll
        for (int m = 0; m < 4; ++m) af[0][m] = *(const u32x4*)(rd + (j + 1) * 8192 + m * 1024);
      } else {
        char* xb = smem + kh * 65536 + cw * 12288 + l16;
#pragma unroll
        for (int m = 0; m < 4; ++m)
#pragma unroll
          for (int u = 0; u < 3; ++u) *(f32x4*)(xb + (m * 3 + u) * 1024) = acc[m][u];
      }
#pragma unroll
      for (int m = 0; m < 4; ++m)
#pragma unroll
        for (int u = 0; u < 3; ++u) acc[4 + m][u] = mfma16(wf[j][u], af[1][m], acc[4 + m][u]);
      if (j == 7) {
#pragma unroll
        for (int i = 0; i < 12; ++i) { __builtin_amdgcn_sched_group_barrier(0x8, 1, 0); __builtin_amdgcn_sched_group_barrier(0x200, 1, 0); }
      }
      if (j < 7) {
#pragma unroll
        for (int i = 0; i < 4; ++i) { __builtin_amdgcn_sched_group_barrier(0x8, 1, 0); __builtin_amdgcn_sched_group_barrier(0x100, 1, 0); }
        __builtin_amdgcn_sched_group_barrier(0x8, 8, 0);
      }
      __builtin_amdgcn_sched_barrier(0);
      if (j >= 2 && j < 7) { WD_LOAD(3 * j - 6); WD_LOAD(3 * j - 5); WD_LOAD(3 * j - 4); }
      if (j == 7) { WD_LOAD(15); }
    }
    __builtin_amdgcn_sched_barrier(0);
  }
  {
    wg_bar();
    const char* xr = smem + (1 - kh) * 65536 + cw * 12288 + l16;
    char* stg = smem + kh * 65536 + 49152 + cw * 3072;
    char* sw_base = stg + i16 * 16 + (4 * (g & 1) + 2 * (g >> 1)) * 2;
    f32x4 px[2][3];
#pragma unroll
    for (int u = 0; u < 3; ++u) px[0][u] = *(const f32x4*)(xr + u * 1024);
#pragma unroll
    for (int m = 0; m < 4; ++m) {
      __builtin_amdgcn_sched_barrier(0);
      if (m < 3) {
#pragma unroll
        for (int u = 0; u < 3; ++u) px[(m + 1) & 1][u] = *(const f32x4*)(xr + ((m + 1) * 3 + u) * 1024);
      }
#pragma unroll
      for (int u = 0; u < 3; ++u) {
        const f32x4 v = acc[4 + m][u] + px[m & 1][u];
        float r[2];
#pragma unroll
        for (int e = 0; e < 2; ++e) {
          const auto sw = __builtin_amdgcn_permlane32_swap(__float_as_uint(v[e]), __float_as_uint(v[2 + e]), false, false);
          const float gt = __uint_as_float(sw[0]), up = __uint_as_float(sw[1]);
          r[e] = (gt * up) * __builtin_amdgcn_rcpf(1.f + __builtin_amdgcn_exp2f(-1.4426950408889634f * gt));
        }
        *(unsigned*)(sw_base + (m * 3 + u) * 256) = pack2(r[0], r[1]);
      }
    }
    __builtin_amdgcn_sched_barrier(0);
#pragma unroll
    for (int i = 0; i < 3; ++i) {
      const int q = i * 4 + g, m = (q * 11) >> 5, hq = h0 + q - 3 * m;
      const u32x4 v = *(const u32x4*)(stg + i * 1024 + l16);
      if (hq >= slot * 3)
        st_dev(rs, (unsigned)((((mt * 8 + 4 * kh + m) * KS4 + (hq >> 2)) * 4 + (hq & 3)) * 16 + i16) * 16u, (unsigned)WS_ACT, v);
    }
    asm volatile("s_waitcnt vmcnt(0)\n\ts_barrier" ::: "memory");
    if (w == 0 && lane == 0) st_tag(ws + WS_FA + (mt * 16 + cg) * 8, tag);
  }
  WD_LOAD(16); WD_LOAD(17); WD_LOAD(18); WD_LOAD(19);
  if (w < 3) { WD_LOAD(20); WD_LOAD(21); WD_LOAD(22); WD_LOAD(23); }
  if (w == 0) wait_tags<1, 1>(ws + WS_FA + ((mt4 >> 1) * 16 + i16) * 8, true, tag);
  __builtin_amdgcn_s_barrier();
  asm volatile("" ::: "memory");
  f32x4 dacc[4][4];
#pragma unroll
  for (int m = 0; m < 4; ++m)
#pragma unroll
    for (int n = 0; n < 4; ++n) dacc[m][n] = (f32x4){0.f, 0.f, 0.f, 0.f};
  {
    u32x4 a4[6][4];
#define D_LOAD(i_) _Pragma("unroll") for (int m = 0; m < 4; ++m) a4[i_][m] = ld_dev(rs, l16, (unsigned)WS_ACT + (unsigned)((mt4 * 4 + m) * KS4 + k0 + (i_)) * 1024u)
#define D_MMA(i_) _Pragma("unroll") for (int m = 0; m < 4; ++m) _Pragma("unroll") for (int n = 0; n < 4; ++n) dacc[m][n] = mfma16(wd4[i_][n], a4[i_][m], dacc[m][n])
#define D_PIN(i_) asm volatile("" : "+a"(a4[i_][0]), "+a"(a4[i_][1]), "+a"(a4[i_][2]), "+a"(a4[i_][3]))
    D_LOAD(0); D_LOAD(1); D_LOAD(2); D_LOAD(3);
    __builtin_amdgcn_sched_barrier(0);
    D_MMA(0);
    __builtin_amdgcn_sched_barrier(0);
    D_LOAD(4);
    __builtin_amdgcn_sched_barrier(0);
    D_PIN(1); D_MMA(1);
    __builtin_amdgcn_sched_barrier(0);
    if (w < 3) { D_LOAD(5); }
    __builtin_amdgcn_sched_barrier(0);
    D_PIN(2); D_MMA(2); D_PIN(3); D_MMA(3); D_MMA(4);
    if (w < 3) { D_MMA(5); }
  }
  const int tid = w * 64 + fresh_lane();
  const int otok = tid >> 3, oc = (tid & 7) * 8;
  const unsigned ooff = (unsigned)((mt4 * 64 + otok) * 512 + nb * 64 + oc);
  const u32x4 hres = ld_dev(rs, ooff * 2u, (unsigned)WS_H);
#pragma unroll
  for (int m = 0; m < 4; ++m)
#pragma unroll
    for (int n = 0; n < 4; ++n)
      *(f32x4*)(smem + (w * 64 + 16 * m + i16) * K4_ROW + (16 * n + 4 * g) * 4) = dacc[m][n];
  wg_bar();
  f32x4 s0 = *(const f32x4*)(smem + otok * K4_ROW + oc * 4), s1 = *(const f32x4*)(smem + otok * K4_ROW + oc * 4 + 16);
#pragma unroll
  for (int q = 1; q < 8; ++q) {
    s0 += *(const f32x4*)(smem + (q * 64 + otok) * K4_ROW + oc * 4);
    s1 += *(const f32x4*)(smem + (q * 64 + otok) * K4_ROW + oc * 4 + 16);
  }
  u32x4 o;
  o[0] = pack2(bflo(hres[0]) + bfr(s0[0]), bfhi(hres[0]) + bfr(s0[1]));
  o[1] = pack2(bflo(hres[1]) + bfr(s0[2]), bfhi(hres[1]) + bfr(s0[3]));
  o[2] = pack2(bflo(hres[2]) + bfr(s1[0]), bfhi(hres[2]) + bfr(s1[1]));
  o[3] = pack2(bflo(hres[3]) + bfr(s1[2]), bfhi(hres[3]) + bfr(s1[3]));
  *(u32x4*)(out + ooff) = o;
  if (blockIdx.x == 0 && tid == 0) st_tag(ws + WS_CNT, tag);
}

DI void k2_helper(char* smem, char* __restrict__ ws, const u16* __restrict__ x, const u16* __restrict__ wg,
                  const u16* __restrict__ wu, const u16* __restrict__ wd, int b, int hw, u32x4 (&wfb)[8][3], u64 tag) {
  const int lane = fresh_lane();
  const int g = lane >> 4;
  lds_vu32* fl = (lds_vu32*)LDSP(smem + K2_OFF_FLAG);
  {
    const u32x4 q0 = *(const u32x4*)(smem + 65536 + ((b + 1 + hw) & 7) * 1024 + lane * 16);
    const u32x4 q1 = *(const u32x4*)(smem + 65536 + ((b + 5 + hw) & 7) * 1024 + lane * 16);
    {
      lds_vu32* kvok = (lds_vu32*)LDSP(smem + K1_OFF_SYNC + 12);
      while (__builtin_amdgcn_readfirstlane(*kvok) == 0u) __builtin_amdgcn_s_sleep(1);
      asm volatile("" ::: "memory");
    }
    if (hw == 0) fl[lane] = 0u;
    *(u32x4*)(smem + K2_OFF_Q + hw * 1024 + lane * 16) = q0;
    *(u32x4*)(smem + K2_OFF_Q + (hw + 4) * 1024 + lane * 16) = q1;
  }
  __builtin_amdgcn_s_setprio(2);
  wg_bar();
  if (hw == 0) {
    const char* nrm = ws + WS_NRM;
    const float qa_ = *(const float*)(smem + K1_OFF_SYNC + 32 + (lane & 3) * 4);
    const float ka = ld_dev4(nrm + (1024 + lane) * 4), kb_ = ld_dev4(nrm + (1088 + lane) * 4);
    const float va = ld_dev4(nrm + (1152 + lane) * 4), vb_ = ld_dev4(nrm + (1216 + lane) * 4);
    const unsigned uq = __float_as_uint(max_row(qa_));
    const unsigned uk = __float_as_uint(max_row(__uint_as_float(max(__float_as_uint(ka), __float_as_uint(kb_)))));
    const unsigned uv = __float_as_uint(max_row(__uint_as_float(max(__float_as_uint(va), __float_as_uint(vb_)))));
#define RL4(u_) max(max((unsigned)__builtin_amdgcn_readlane((int)(u_), 0), (unsigned)__builtin_amdgcn_readlane((int)(u_), 16)), \
                  max((unsigned)__builtin_amdgcn_readlane((int)(u_), 32), (unsigned)__builtin_amdgcn_readlane((int)(u_), 48)))
    const bool safe = (__uint_as_float(RL4(uq)) * __uint_as_float(RL4(uk)) * 1.05f < 3600.f) && (__uint_as_float(RL4(uv)) < 1e12f);
    if (lane < 4) fl[40 + lane] = safe ? 1u : 2u;
  }
  else __builtin_amdgcn_s_sleep(24);
  wait_tags<3, 8>(ws + WS_FO + min(lane, 7) * 8, true, tag);
  wait_tags<2, 8>(ws + WS_FO + lane * 8, true, tag);
  {
    const char* wp = ws + WS_WOF + (size_t)(blockIdx.x >> 3) * 16384 + hw * 4096 + (unsigned)(lane * 16);
#pragma unroll
    for (int i = 0; i < 4; ++i) dma16_dev(wp + i * 1024, smem + K2_OFF_DUMP + hw * 1024);
  }
  const int grp = (int)blockIdx.x * 4 + hw;
  retile_issue(wg, wu, wd, wd, grp, lane, smem + K2_OFF_IMG + hw * 4096);
  const int tok = lane & 7, ch = (lane >> 3) & 1;
#pragma unroll
  for (int i = 0; i < 2; ++i)
    dma16((const char*)x + (size_t)(b * 8 + i * 4 + g) * 1024 + 256 * hw + (lane & 15) * 16, smem + K2_OFF_X + hw * 2048 + i * 1024);

  const __amdgpu_buffer_rsrc_t rso = ws_rsrc(ws);
  const char* ob = smem + K2_OFF_OBUF + (g * 8 + tok) * 16;
  f32x4 oacc[8];
#pragma unroll
  for (int i = 0; i < 8; ++i) oacc[i] = (f32x4){0.f, 0.f, 0.f, 0.f};
  auto otile = [&](int i, int red) __attribute__((always_inline)) {
    const int t = i - 2, src = (b - 1 - t) & 7;
    u32x4 wf[2][8];
#pragma unroll
    for (int ksp = 0; ksp < 2; ++ksp)
#pragma unroll
      for (int ct = 0; ct < 8; ++ct)
        wf[ksp][ct] = ld_dev(rso, (unsigned)(lane * 16) + (ct & 3) * 1024, (unsigned)WS_WOF + (unsigned)(((2 * src + ksp) * 32 + hw * 8 + (ct & 4)) * 1024));
    asm volatile("" ::: "memory");
    if (red >= 0 && hw == (red & 3)) k2_reduce(smem, ws, b, red, lane, tag);
    while (__builtin_amdgcn_readfirstlane(fl[32 + t]) == 0u) { if (t < 4) __builtin_amdgcn_s_sleep(8); else __builtin_amdgcn_s_sleep(1); }
    asm volatile("" ::: "memory");
    const u32x4 of0 = *(const u32x4*)(ob + t * 1024), of1 = *(const u32x4*)(ob + t * 1024 + 512);
#pragma unroll
    for (int ct = 0; ct < 8; ++ct) oacc[ct] = mfma16(wf[0][ct], of0, oacc[ct]);
#pragma unroll
    for (int ct = 0; ct < 8; ++ct) oacc[ct] = mfma16(wf[1][ct], of1, oacc[ct]);
    if (t < 6 && hw == ((t + 3) & 3)) k2_receive(smem, ws, b, t + 1, lane, tag);
  };
  {
    asm volatile("s_waitcnt vmcnt(0)" ::: "memory");
    retile_finish(ws, grp, lane, smem + K2_OFF_IMG + hw * 4096);
    asm volatile("s_waitcnt vmcnt(0)" ::: "memory");
    if (lane == 0 && lds_inc(smem + K2_OFF_FLAG + 176) == 3u) st_tag(ws + WS_FW + blockIdx.x * 8, tag);
  }
  if (hw < 2) k2_reduce(smem, ws, b, hw, lane, tag);
  if (hw == 3) k2_receive(smem, ws, b, 0, lane, tag);
#pragma unroll 1
  for (int i = 2; i < 4; ++i) otile(i, i);
  {
    wait_tags<4, 8>(ws + WS_FW + lane * 8, true, tag);
    fl[48] = 1u;
    const int idx = blockIdx.x >> 3;
    const __amdgpu_buffer_rsrc_t rsw = ws_rsrc(ws);
#pragma unroll
    for (int k = 0; k < 8; ++k) {
#pragma unroll
      for (int u = 0; u < 3; ++u) dma16_dev(ws + wgu_off(idx, 1, hw, k) + u * 1024 + (unsigned)(lane * 16), smem + K2_OFF_DUMP + hw * 1024);
#pragma unroll
      for (int u = 0; u < 3; ++u) {
        if (k >= 2) wfb[k][u] = ld_dev(rsw, (unsigned)(lane * 16) + u * 1024, wgu_off(idx, 0, hw, k));
        else dma16_dev(ws + wgu_off(idx, 0, hw, k) + u * 1024 + (unsigned)(lane * 16), smem + K2_OFF_DUMP + hw * 1024);
      }
    }
#define PINW(k_) "+a"(wfb[k_][0]), "+a"(wfb[k_][1]), "+a"(wfb[k_][2])
    asm volatile("" : PINW(2), PINW(3), PINW(4), PINW(5), PINW(6), PINW(7));
  }
#pragma unroll 1
  for (int i = 4; i < 8; ++i) {
    if (i == 7) {
      while (__builtin_amdgcn_readfirstlane(fl[52]) < 7u) __builtin_amdgcn_s_sleep(1);
      const char* src = ws + WS_WOF + (size_t)(2 * (b & 7) * 32 + hw * 8) * 1024 + (unsigned)(fresh_lane() * 16);
#pragma unroll 1
      for (int q = 0; q < 16; ++q) dma16_dev(src + ((q >> 3) * 32 + (q & 7)) * 1024, smem + hw * 16384 + q * 1024);
    }
    otile(i, i < 7 ? i : -1);
  }
  {
    const int src = (b - 7) & 7;
    u32x4 wf[2][8];
#pragma unroll
    for (int ksp = 0; ksp < 2; ++ksp)
#pragma unroll
      for (int ct = 0; ct < 8; ++ct)
        wf[ksp][ct] = ld_dev(rso, (unsigned)(lane * 16) + (ct & 3) * 1024, (unsigned)WS_WOF + (unsigned)(((2 * src + ksp) * 32 + hw * 8 + (ct & 4)) * 1024));
    asm volatile("" ::: "memory");
    if (hw == 3) k2_reduce(smem, ws, b, 7, lane, tag);
    while (__builtin_amdgcn_readfirstlane(fl[39]) == 0u) __builtin_amdgcn_s_sleep(1);
    asm volatile("" ::: "memory");
#pragma unroll
    for (int ksp = 0; ksp < 2; ++ksp) {
      const u32x4 of = *(const u32x4*)(ob + 7 * 1024 + ksp * 512);
#pragma unroll
      for (int ct = 0; ct < 8; ++ct) {
        oacc[ct] = mfma16(*(const u32x4*)(smem + hw * 16384 + (ksp * 8 + ct) * 1024 + lane * 16), of, oacc[ct]);
        if ((ct & 3) == 3) __builtin_amdgcn_sched_barrier(0);
      }
    }
    while (__builtin_amdgcn_readfirstlane(fl[38]) == 0u) __builtin_amdgcn_s_sleep(1);
    asm volatile("" ::: "memory");
    const u32x4 of0 = *(const u32x4*)(ob + 6 * 1024), of1 = *(const u32x4*)(ob + 6 * 1024 + 512);
#pragma unroll
    for (int ct = 0; ct < 8; ++ct) oacc[ct] = mfma16(wf[0][ct], of0, oacc[ct]);
#pragma unroll
    for (int ct = 0; ct < 8; ++ct) oacc[ct] = mfma16(wf[1][ct], of1, oacc[ct]);
  }
  f32x2* red = (f32x2*)(smem + K2_OFF_RED);
  u32x2 xv[4];
#pragma unroll
  for (int ct = 0; ct < 4; ++ct) xv[ct] = *(const u32x2*)(smem + K2_OFF_X + hw * 2048 + tok * 256 + (64 * ch + 16 * ct + 4 * g) * 2);
  float hv[4][4];
  float s1 = 0.f;
#pragma unroll
  for (int ct = 0; ct < 4; ++ct) {
    f32x4 o;
#pragma unroll
    for (int r = 0; r < 4; ++r) o[r] = ch ? oacc[4 + ct][r] : oacc[ct][r];
    hv[ct][0] = bfr(bflo(xv[ct][0]) + bfr(o[0]));
    hv[ct][1] = bfr(bfhi(xv[ct][0]) + bfr(o[1]));
    hv[ct][2] = bfr(bflo(xv[ct][1]) + bfr(o[2]));
    hv[ct][3] = bfr(bfhi(xv[ct][1]) + bfr(o[3]));
    s1 += (hv[ct][0] + hv[ct][1]) + (hv[ct][2] + hv[ct][3]);
  }
  s1 = sum_g(s1);
  const float mw = s1 * (1.f / 64.f);
  float s2 = 0.f;
#pragma unroll
  for (int ct = 0; ct < 4; ++ct)
#pragma unroll
    for (int r = 0; r < 4; ++r) { const float d = hv[ct][r] - mw; s2 += d * d; }
  s2 = sum_g(s2);
  if (lane < 16) red[tok * 8 + hw * 2 + ch] = (f32x2){mw, s2};
  wg_bar();
  f32x4 rv[4];
#pragma unroll
  for (int i = 0; i < 4; ++i) rv[i] = *(const f32x4*)(red + tok * 8 + 2 * i);
  const float mean = (((rv[0][0] + rv[0][2]) + (rv[1][0] + rv[1][2])) + ((rv[2][0] + rv[2][2]) + (rv[3][0] + rv[3][2]))) * 0.125f;
  float m2 = 0.f, dv = 0.f;
#pragma unroll
  for (int i = 0; i < 4; ++i) {
    m2 += rv[i][1] + rv[i][3];
    const float d0 = rv[i][0] - mean, d1 = rv[i][2] - mean;
    dv += d0 * d0 + d1 * d1;
  }
  const float var = (m2 + 64.f * dv) * (1.f / 512.f);
  const float mb = bfr(mean);
  const float sb = bfr(__builtin_amdgcn_sqrtf(bfr(bfr(var) + 1e-5f)));
  const float rs = __builtin_amdgcn_rcpf(sb);
  {
    char* sg = smem + K2_OFF_IMG + hw * 4096;
#pragma unroll
    for (int ct = 0; ct < 4; ++ct) {
      float n[4];
#pragma unroll
      for (int r = 0; r < 4; ++r) n[r] = bfr(hv[ct][r] - mb) * rs;
      const int ksl = 2 * ch + (ct >> 1), gg = (ct & 1) * 2 + (g >> 1);
      *(u32x2*)(sg + ((ksl * 4 + gg) * 8 + tok) * 16 + 8 * (g & 1)) = (u32x2){pack2(n[0], n[1]), pack2(n[2], n[3])};
      *(u32x2*)(sg + 2048 + tok * 256 + (64 * ch + 16 * ct + 4 * g) * 2) = (u32x2){pack2(hv[ct][0], hv[ct][1]), pack2(hv[ct][2], hv[ct][3])};
    }
    const __amdgpu_buffer_rsrc_t rsx = ws_rsrc(ws);
    const int tt = b >> 1, t8 = (b & 1) * 8;
#pragma unroll
    for (int i = 0; i < 2; ++i) {
      const int ci = i * 64 + lane;
      const u32x4 v = *(const u32x4*)(sg + ci * 16);
      st_dev(rsx, (unsigned)(((((4 * hw + (ci >> 5)) * 128 + tt) * 4 + ((ci >> 3) & 3)) * 16 + t8 + (ci & 7)) * 16), (unsigned)WS_HN, v);
    }
    asm volatile("s_waitcnt vmcnt(0)\n\ts_barrier" ::: "memory");
    if (hw == 0 && lane == 0) st_tag(ws + WS_FH + b * 8, tag);
#pragma unroll
    for (int i = 0; i < 2; ++i) {
      const int ci = i * 64 + lane;
      const u32x4 v = *(const u32x4*)(sg + 2048 + ci * 16);
      st_dev(rsx, (unsigned)((b * 8 + (ci >> 4)) * 1024 + 256 * hw + (ci & 15) * 16), (unsigned)WS_H, v);
    }
  }
  __builtin_amdgcn_s_setprio(0);
  asm volatile("" : PINW(2), PINW(3), PINW(4), PINW(5), PINW(6), PINW(7));
#define PINV(k_) "+v"(wfb[k_][0]), "+v"(wfb[k_][1]), "+v"(wfb[k_][2])
  asm volatile("" : PINV(2), PINV(3), PINV(4), PINV(5));
  {
    const __amdgpu_buffer_rsrc_t rsw = ws_rsrc(ws);
#pragma unroll
    for (int k = 0; k < 2; ++k)
#pragma unroll
      for (int u = 0; u < 3; ++u) wfb[k][u] = ld_dev(rsw, (unsigned)(lane * 16) + u * 1024, wgu_off(blockIdx.x >> 3, 0, hw, k));
  }
}

__global__ __launch_bounds__(512, 2) void k2_fused(const u16* __restrict__ x, const u16* __restrict__ wq,
                                                   const u16* __restrict__ wk, const u16* __restrict__ wv,
                                                   const u16* __restrict__ wo, const u16* __restrict__ wg,
                                                   const u16* __restrict__ wu, const u16* __restrict__ wd,
                                                   char* __restrict__ ws, u16* __restrict__ out) {
  extern __shared__ __attribute__((aligned(16))) char smem[];
  const int w = __builtin_amdgcn_readfirstlane(threadIdx.x >> 6);
  const int xcd = blockIdx.x & 7, idx = blockIdx.x >> 3;
  const int b = xcd * 32 + idx;
  const u64 tag = *(const __attribute__((address_space(4))) u64*)(ws + WS_CNT) + 1ull;
  const int cb = idx & 7, rt = xcd * 4 + (idx >> 3);
  u32x4 wf[8][3];
  if (w >= 4) {
    const int aw = w - 4;
    kv_wave(smem, wk, wv, wo, wd, ws, aw, rt, cb, tag);
    asm volatile("s_waitcnt vmcnt(0)" ::: "memory");
    const int lane = fresh_lane(), c = lane & 31, hh = lane >> 5;
    const unsigned lb = (unsigned)(size_t)LDSP(smem);
    unsigned pa = lb + K2_OFF_PART + aw * PART_STRIDE + c * PROW + 16 * hh;
    unsigned sf = lane * 16;
    unsigned qa = lb + K2_OFF_Q + lane * 16;
    unsigned fa = lb + K2_OFF_FLAG + aw * 4;
    unsigned cst = (c == 16) ? 0x3f803f80u : 0u;
    const char* kb = ws + WS_K + (size_t)aw * 16384;
    const char* vb = ws + WS_VF + (size_t)aw * 16384;
    unsigned long long t0, t1;
    unsigned ok, cnt;
    asm volatile(ATTN_WAVE_ASM
                 : "+{v0}"(pa), "+{v1}"(sf), "+{v2}"(qa), "+{v3}"(fa), "+{v26}"(cst),
                   [t0] "=&s"(t0), [t1] "=&s"(t1), [ok] "=&s"(ok), [cnt] "=&s"(cnt)
                 : [kb] "s"(kb), [vb] "s"(vb), [pstep] "s"(4 * PART_STRIDE), [msk] "s"(0x0000ffff0000ffffull)
                 : "memory", "scc", "v24", "v25", ATTN_WAVE_CLOB);
    if (ok != 1u) k2_attn_slow(smem, ws, aw);
    {
      lds_vu32* fl = (lds_vu32*)LDSP(smem + K2_OFF_FLAG);
      while (__builtin_amdgcn_readfirstlane(fl[48]) == 0u) __builtin_amdgcn_s_sleep(2);
      asm volatile("" ::: "memory");
    }
    {
      const unsigned l16 = (unsigned)fresh_lane() * 16u;
      const __amdgpu_buffer_rsrc_t rs = ws_rsrc(ws);
#pragma unroll
      for (int k = 0; k < 8; ++k)
#pragma unroll
        for (int u = 0; u < 3; ++u) wf[k][u] = ld_dev(rs, l16 + u * 1024, wgu_off(idx, 1, aw, k));
    }
    __builtin_amdgcn_s_barrier();
    __builtin_amdgcn_s_barrier();
    asm volatile("" ::: "memory");
  } else {
    q_wave(smem, x, wq, ws, w, rt, cb, tag);
    k2_helper(smem, ws, x, wg, wu, wd, b, w, wf, tag);
  }
  ffn_phases(smem, ws, out, w, xcd, idx, wf, tag);
}

extern "C" void kernel_launch(void* const* d_in, const int* in_sizes, int n_in,
                              void* d_out, int out_size, void* d_ws, size_t ws_size,
                              hipStream_t stream) {
  const u16* x = (const u16*)d_in[0];
  const u16* wq = (const u16*)d_in[1];
  const u16* wk = (const u16*)d_in[2];
  const u16* wv = (const u16*)d_in[3];
  const u16* wo = (const u16*)d_in[4];
  const u16* wg = (const u16*)d_in[6];
  const u16* wu = (const u16*)d_in[7];
  const u16* wd = (const u16*)d_in[8];
  char* ws = (char*)d_ws;
  (void)hipFuncSetAttribute((const void*)k2_fused, hipFuncAttributeMaxDynamicSharedMemorySize, KM_LDS);
  k2_fused<<<256, 512, KM_LDS, stream>>>(x, wq, wk, wv, wo, wg, wu, wd, ws, (u16*)d_out);
}
```
